```python
import torch

def _softmax(x, axis=-1):
    return torch.softmax(x, dim=axis)
def _gelu(x):
    return torch.nn.functional.gelu(x, approximate="tanh")
def _silu(x):
    return torch.nn.functional.silu(x)
def _sigmoid(x):
    return torch.sigmoid(x)
def _layernorm(x, axis=-1, eps=1e-5):
    m = x.mean(dim=axis, keepdim=True)
    v = x.var(dim=axis, keepdim=True, unbiased=False)
    return (x - m) / torch.sqrt(v + eps)
def _scatter_add(tgt, idx, src, axis=0):
    return tgt.clone().index_add_(axis, idx, src)
def _segment_sum(src, seg, n):
    out = torch.zeros((n,) + src.shape[1:], dtype=src.dtype)
    return out.index_add_(0, seg, src)
def _pad(x, pw):
    flat = tuple(v for pair in reversed(pw) for v in pair)
    return torch.nn.functional.pad(x, flat)

kernel_name = 'gated_mix__gqa_block_cached__glu_gated_geglu__decode'

def setup_inputs():

    _w = (lambda a: (lambda t: t.to(torch.bfloat16) if t.is_floating_point() else t)(torch.as_tensor(a)))
    def _A_setup():
        B, Sq, D = 2, 1, 576
        Hq, Hkv, Sk, Dh = 32, 16, 32768, 128
        assert Hq % Hkv == 0, (Hq, Hkv)
        return {
            "x": _w((lambda *s: torch.randn(*s, dtype=torch.bfloat16) * 0.05)(B, Sq, D)),
            "k_cache": _w((lambda *s: torch.randn(*s, dtype=torch.bfloat16) * 6.4)(B, Hkv, Sk, Dh)),
            "v_cache": _w((lambda *s: torch.randn(*s, dtype=torch.bfloat16) * 0.05)(B, Hkv, Sk, Dh)),
            "wq": _w((lambda *s: torch.randn(*s, dtype=torch.bfloat16) * 6.4)(D, Hq * Dh) * (D ** -0.5)),
            "wo": _w((lambda *s: torch.randn(*s, dtype=torch.bfloat16) * 0.05)(Hq * Dh, D) * ((Hq * Dh) ** -0.5)),
        }
    _A_in = _A_setup()

    def _B_setup():
        B, S, Dm, Df = 2, 1, 576, 4608
        return {
            "x": _w((lambda *s: torch.randn(*s, dtype=torch.bfloat16) * 0.05)(B, S, Dm)),
            "w_gate": _w((lambda *s: torch.randn(*s, dtype=torch.bfloat16) * 0.4)(Dm, Df) * (Dm ** -0.5)),
            "w_up": _w((lambda *s: torch.randn(*s, dtype=torch.bfloat16) * 0.4)(Dm, Df) * (Dm ** -0.5)),
            "w_down": _w((lambda *s: torch.randn(*s, dtype=torch.bfloat16) * 0.4)(Df, Dm) * (Df ** -0.5)),
        }
    _B_in = _B_setup()

    return {
        "A_x": _A_in["x"],
        "A_k_cache": _A_in["k_cache"],
        "A_v_cache": _A_in["v_cache"],
        "A_wq": _A_in["wq"],
        "A_wo": _A_in["wo"],
        "B_x": _B_in["x"],
        "B_w_gate": _B_in["w_gate"],
        "B_w_up": _B_in["w_up"],
        "B_w_down": _B_in["w_down"],
        "gate_w": _w((lambda *s: torch.randn(*s, dtype=torch.bfloat16) * 0.05)(576) * 16.0),
    }

def reference(A_x, A_k_cache, A_v_cache, A_wq, A_wo, B_x, B_w_gate, B_w_up, B_w_down, gate_w):
    def _A_ref(x, k_cache, v_cache, wq, wo):
        B, Sq, D = x.shape
        Hq, Dh = 32, 128
        Hkv = k_cache.shape[1]
        rep = Hq // Hkv
        q = (lambda x, *d: x.permute(*d))((x @ wq).reshape(B, Sq, Hq, Dh), 0, 2, 1, 3)
        k = (lambda x, n, axis: torch.repeat_interleave(x, n, dim=axis))(k_cache, rep, axis=1)
        v = (lambda x, n, axis: torch.repeat_interleave(x, n, dim=axis))(v_cache, rep, axis=1)
        scores = torch.einsum("bhqd,bhkd->bhqk", q, k) * (Dh ** -0.5)
        p = _softmax(scores, axis=-1)
        o = torch.einsum("bhqk,bhkd->bhqd", p, v)
        return (lambda x, *d: x.permute(*d))(o, 0, 2, 1, 3).reshape(B, Sq, Hq * Dh) @ wo

    def _B_ref(x, w_gate, w_up, w_down):
        g = torch.einsum("bsd,df->bsf", x, w_gate)
        u = torch.einsum("bsd,df->bsf", x, w_up)
        h = _gelu(g) * u
        return torch.einsum("bsf,fd->bsd", h, w_down)

    x = A_x
    g = _sigmoid((x * gate_w).sum(-1, keepdims=True))
    a = _A_ref(x, A_k_cache, A_v_cache, A_wq, A_wo)
    b = _B_ref(x, B_w_gate, B_w_up, B_w_down)
    return g * a + (1 - g) * b
```

```cpp
#include <hip/hip_runtime.h>
#include <hip/hip_bf16.h>
#include <cstdint>

constexpr int NB = 2, DM = 576, HQ = 32, HKV = 16, SK = 32768, DH = 128, DF = 4608;
constexpr int QN = HQ * DH;
constexpr int NSPLIT = 16;
constexpr int WAVES = 4, PAGE = 16;
constexpr int TOK_PER_WAVE = SK / NSPLIT / WAVES;
constexpr int PPW = TOK_PER_WAVE / PAGE;
constexpr float SCALE = 0.088388347648318440f;
constexpr float LOG2E = 1.4426950408889634f;

constexpr size_t WS_CNT = 0;
constexpr size_t WS_Q = 4096;
constexpr size_t WS_G = WS_Q + (size_t)NB * QN * 2;
constexpr size_t WS_U = WS_G + (size_t)NB * DF * 4;
constexpr size_t WS_PML = WS_U + (size_t)NB * DF * 4;
constexpr size_t WS_PO = WS_PML + (size_t)NB * HQ * NSPLIT * 2 * 4;
constexpr size_t WS_FP = WS_PO + (size_t)NB * HQ * NSPLIT * DH * 4;

using bf16x2 = __attribute__((ext_vector_type(2))) __bf16;
using u32x4 = __attribute__((ext_vector_type(4))) unsigned;
using f32x4 = __attribute__((ext_vector_type(4))) float;

__device__ __forceinline__ float dot2(unsigned k, unsigned q, float acc) {
  return __builtin_amdgcn_fdot2_f32_bf16(__builtin_bit_cast(bf16x2, k), __builtin_bit_cast(bf16x2, q), acc, false);
}
__device__ __forceinline__ float bf_lo(unsigned w) { return __builtin_bit_cast(float, w << 16); }
__device__ __forceinline__ float bf_hi(unsigned w) { return __builtin_bit_cast(float, w & 0xffff0000u); }
__device__ __forceinline__ float bf2f(unsigned short h) { return __builtin_bit_cast(float, (unsigned)h << 16); }
__device__ __forceinline__ unsigned short f2bf(float f) {
  unsigned u = __builtin_bit_cast(unsigned, f);
  u += 0x7fffu + ((u >> 16) & 1u);
  return (unsigned short)(u >> 16);
}
__device__ __forceinline__ float rbf(float f) { return bf2f(f2bf(f)); }
template <int CTRL> __device__ __forceinline__ float dpp(float x) {
  return __builtin_bit_cast(float, __builtin_amdgcn_mov_dpp(__builtin_bit_cast(int, x), CTRL, 0xf, 0xf, true));
}
constexpr int XOR1 = 0xB1, XOR2 = 0x4E, XOR7 = 0x141, XOR8 = 0x128;
__device__ __forceinline__ float row16_allsum(float x) {
  x += dpp<XOR8>(x); x += dpp<XOR7>(x); x += dpp<XOR2>(x); x += dpp<XOR1>(x); return x;
}

__global__ void __launch_bounds__(256) proj_kernel(const unsigned short* __restrict__ x, const unsigned short* __restrict__ wq,
                                                   const unsigned short* __restrict__ wg, const unsigned short* __restrict__ wu,
                                                   unsigned char* __restrict__ ws) {
  __shared__ float xs[NB][DM];
  __shared__ float red[4][NB][64];
  const int tid = threadIdx.x, blk = blockIdx.x;
  if (blk == 0 && tid < 64) reinterpret_cast<unsigned*>(ws + WS_CNT)[tid * 16] = 0u;
  for (int i = tid; i < NB * DM; i += 256) xs[i / DM][i % DM] = bf2f(x[i]);
  const unsigned short* w; int ncol, c0, which;
  if (blk < QN / 64) { w = wq; ncol = QN; c0 = blk * 64; which = 0; }
  else if (blk < QN / 64 + DF / 64) { w = wg; ncol = DF; c0 = (blk - QN / 64) * 64; which = 1; }
  else { w = wu; ncol = DF; c0 = (blk - QN / 64 - DF / 64) * 64; which = 2; }
  const int cg = tid & 7, kl = tid >> 3;
  u32x4 wv[18];
#pragma unroll
  for (int i = 0; i < 18; ++i) wv[i] = *reinterpret_cast<const u32x4*>(w + (size_t)(kl + 32 * i) * ncol + c0 + cg * 8);
  __syncthreads();
  float a0[8], a1[8];
#pragma unroll
  for (int j = 0; j < 8; ++j) { a0[j] = 0.f; a1[j] = 0.f; }
#pragma unroll
  for (int i = 0; i < 18; ++i) {
    const float x0 = xs[0][kl + 32 * i], x1 = xs[1][kl + 32 * i];
#pragma unroll
    for (int j = 0; j < 4; ++j) {
      const float lo = bf_lo(wv[i][j]), hi = bf_hi(wv[i][j]);
      a0[2 * j] = fmaf(x0, lo, a0[2 * j]); a0[2 * j + 1] = fmaf(x0, hi, a0[2 * j + 1]);
      a1[2 * j] = fmaf(x1, lo, a1[2 * j]); a1[2 * j + 1] = fmaf(x1, hi, a1[2 * j + 1]);
    }
  }
#pragma unroll
  for (int j = 0; j < 8; ++j) {
    a0[j] += __shfl_xor(a0[j], 8); a0[j] += __shfl_xor(a0[j], 16); a0[j] += __shfl_xor(a0[j], 32);
    a1[j] += __shfl_xor(a1[j], 8); a1[j] += __shfl_xor(a1[j], 16); a1[j] += __shfl_xor(a1[j], 32);
  }
  const int wave = tid >> 6, lane = tid & 63;
  if (lane < 8) {
#pragma unroll
    for (int j = 0; j < 8; ++j) { red[wave][0][lane * 8 + j] = a0[j]; red[wave][1][lane * 8 + j] = a1[j]; }
  }
  __syncthreads();
  if (tid < 128) {
    const int b = tid >> 6, c = tid & 63;
    const float s = (red[0][b][c] + red[1][b][c]) + (red[2][b][c] + red[3][b][c]);
    if (which == 0) reinterpret_cast<unsigned short*>(ws + WS_Q)[b * QN + c0 + c] = f2bf(s);
    else if (which == 1) reinterpret_cast<float*>(ws + WS_G)[b * DF + c0 + c] = s;
    else reinterpret_cast<float*>(ws + WS_U)[b * DF + c0 + c] = s;
  }
}

__global__ void __launch_bounds__(256, 2) attn_kernel(const unsigned short* __restrict__ kc, const unsigned short* __restrict__ vc,
                                                      unsigned char* __restrict__ ws) {
  __shared__ __attribute__((aligned(16))) float mrg[16][2][4 + DH];
  const int s = blockIdx.x, kvh = blockIdx.y, b = blockIdx.z;
  const int tid = threadIdx.x, lane = tid & 63, wave = __builtin_amdgcn_readfirstlane(tid >> 6), r = lane >> 4, c = lane & 15;
  constexpr float C2 = SCALE * LOG2E;
  const unsigned short* qp = reinterpret_cast<const unsigned short*>(ws + WS_Q) + b * QN + kvh * 2 * DH + 8 * c;
  const u32x4 q0 = *reinterpret_cast<const u32x4*>(qp), q1 = *reinterpret_cast<const u32x4*>(qp + DH);
  const size_t base = ((size_t)(b * HKV + kvh) * SK + (size_t)s * (SK / NSPLIT) + (size_t)wave * PAGE + r) * DH + 8 * c;
  const u32x4* kp = reinterpret_cast<const u32x4*>(kc + base);
  const u32x4* vp = reinterpret_cast<const u32x4*>(vc + base);
  u32x4 Kn[4], Vn[4];
  const int rot = (s * 2 + kvh * 7 + b * 13);
#pragma unroll
  for (int t = 0; t < 4; ++t) { Kn[t] = __builtin_nontemporal_load(kp + (rot & (PPW - 1)) * (256 * WAVES) + t * 64); Vn[t] = __builtin_nontemporal_load(vp + (rot & (PPW - 1)) * (256 * WAVES) + t * 64); }
  float m0 = -INFINITY, m1 = -INFINITY, l0 = 0.f, l1 = 0.f, acc0[8], acc1[8];
#pragma unroll
  for (int k = 0; k < 8; ++k) { acc0[k] = 0.f; acc1[k] = 0.f; }
  for (int i = 0; i < PPW; ++i) {
    u32x4 Kc[4], Vc[4];
#pragma unroll
    for (int t = 0; t < 4; ++t) { Kc[t] = Kn[t]; Vc[t] = Vn[t]; }
    if (i + 1 < PPW) {
#pragma unroll
      for (int t = 0; t < 4; ++t) { Kn[t] = __builtin_nontemporal_load(kp + ((i + 1 + rot) & (PPW - 1)) * (256 * WAVES) + t * 64); Vn[t] = __builtin_nontemporal_load(vp + ((i + 1 + rot) & (PPW - 1)) * (256 * WAVES) + t * 64); }
    }
    float x0[4], x1[4];
#pragma unroll
    for (int t = 0; t < 4; ++t) {
      float d0 = 0.f, d1 = 0.f;
#pragma unroll
      for (int k = 0; k < 4; ++k) { d0 = dot2(Kc[t][k], q0[k], d0); d1 = dot2(Kc[t][k], q1[k], d1); }
      x0[t] = row16_allsum(d0) * C2; x1[t] = row16_allsum(d1) * C2;
    }
    const float mn0 = fmaxf(fmaxf(fmaxf(x0[0], x0[1]), fmaxf(x0[2], x0[3])), m0);
    const float mn1 = fmaxf(fmaxf(fmaxf(x1[0], x1[1]), fmaxf(x1[2], x1[3])), m1);
    const float al0 = __builtin_amdgcn_exp2f(m0 - mn0), al1 = __builtin_amdgcn_exp2f(m1 - mn1);
    m0 = mn0; m1 = mn1;
    float p0[4], p1[4];
#pragma unroll
    for (int t = 0; t < 4; ++t) { p0[t] = __builtin_amdgcn_exp2f(x0[t] - mn0); p1[t] = __builtin_amdgcn_exp2f(x1[t] - mn1); }
    l0 = fmaf(l0, al0, (p0[0] + p0[1]) + (p0[2] + p0[3]));
    l1 = fmaf(l1, al1, (p1[0] + p1[1]) + (p1[2] + p1[3]));
#pragma unroll
    for (int k = 0; k < 8; ++k) { acc0[k] *= al0; acc1[k] *= al1; }
#pragma unroll
    for (int t = 0; t < 4; ++t) {
#pragma unroll
      for (int k = 0; k < 4; ++k) {
        const float lo = bf_lo(Vc[t][k]), hi = bf_hi(Vc[t][k]);
        acc0[2 * k] = fmaf(p0[t], lo, acc0[2 * k]); acc0[2 * k + 1] = fmaf(p0[t], hi, acc0[2 * k + 1]);
        acc1[2 * k] = fmaf(p1[t], lo, acc1[2 * k]); acc1[2 * k + 1] = fmaf(p1[t], hi, acc1[2 * k + 1]);
      }
    }
  }
  {
    float* rec0 = &mrg[wave * 4 + r][0][0];
    float* rec1 = &mrg[wave * 4 + r][1][0];
    *reinterpret_cast<f32x4*>(rec0 + 4 + 8 * c) = f32x4{acc0[0], acc0[1], acc0[2], acc0[3]};
    *reinterpret_cast<f32x4*>(rec0 + 8 + 8 * c) = f32x4{acc0[4], acc0[5], acc0[6], acc0[7]};
    *reinterpret_cast<f32x4*>(rec1 + 4 + 8 * c) = f32x4{acc1[0], acc1[1], acc1[2], acc1[3]};
    *reinterpret_cast<f32x4*>(rec1 + 8 + 8 * c) = f32x4{acc1[4], acc1[5], acc1[6], acc1[7]};
    if (c == 0) { rec0[0] = m0; rec0[1] = l0; rec1[0] = m1; rec1[1] = l1; }
  }
  __syncthreads();
  {
    const int g = tid >> 7, d = tid & 127;
    float M = -INFINITY;
#pragma unroll
    for (int w = 0; w < 16; ++w) M = fmaxf(M, mrg[w][g][0]);
    float L = 0.f, o = 0.f;
#pragma unroll
    for (int w = 0; w < 16; ++w) { const float wt = __builtin_amdgcn_exp2f(mrg[w][g][0] - M); L = fmaf(wt, mrg[w][g][1], L); o = fmaf(wt, mrg[w][g][4 + d], o); }
    const size_t slot = ((size_t)(b * HQ + kvh * 2 + g)) * NSPLIT + s;
    reinterpret_cast<float*>(ws + WS_PO)[slot * DH + d] = o;
    if (d == 0) { float* pml = reinterpret_cast<float*>(ws + WS_PML) + slot * 2; pml[0] = M; pml[1] = L; }
  }
}

__global__ void __launch_bounds__(192) final_kernel(const unsigned short* __restrict__ x, const unsigned short* __restrict__ wo,
                                                    const unsigned short* __restrict__ wd, const unsigned short* __restrict__ gw,
                                                    unsigned char* __restrict__ ws, unsigned short* __restrict__ out) {
  __shared__ float vin[NB][128];
  __shared__ float red[8][NB][192];
  __shared__ unsigned flag;
  const int kb = blockIdx.x, cgp = blockIdx.y, tid = threadIdx.x;
  const int cl = tid % 24, rl = tid / 24;
  const unsigned short* w = (kb < 32 ? wo + (size_t)kb * 128 * DM : wd + (size_t)(kb - 32) * 128 * DM) + cgp * 192 + cl * 8;
  u32x4 wv[16];
#pragma unroll
  for (int i = 0; i < 16; ++i) wv[i] = *reinterpret_cast<const u32x4*>(w + (size_t)(rl + 8 * i) * DM);
  for (int e = tid; e < NB * 128; e += 192) {
    const int b = e >> 7, d = e & 127;
    float v;
    if (kb < 32) {
      const size_t row = (size_t)(b * HQ + kb) * NSPLIT;
      const float* pml = reinterpret_cast<const float*>(ws + WS_PML) + row * 2;
      const float* po = reinterpret_cast<const float*>(ws + WS_PO) + row * DH + d;
      float M = -INFINITY;
      for (int s = 0; s < NSPLIT; ++s) M = fmaxf(M, pml[2 * s]);
      float L = 0.f, o = 0.f;
      for (int s = 0; s < NSPLIT; ++s) { const float wt = __builtin_amdgcn_exp2f(pml[2 * s] - M); L = fmaf(wt, pml[2 * s + 1], L); o = fmaf(wt, po[(size_t)s * DH], o); }
      v = rbf(o / L);
    } else {
      const int f = (kb - 32) * 128 + d;
      const float g = rbf(reinterpret_cast<const float*>(ws + WS_G)[b * DF + f]), u = rbf(reinterpret_cast<const float*>(ws + WS_U)[b * DF + f]);
      const float t = tanhf(0.7978845608028654f * (g + 0.044715f * g * g * g));
      const float ge = rbf(0.5f * g * (1.f + t));
      v = rbf(ge * u);
    }
    vin[b][d] = v;
  }
  __syncthreads();
  float a0[8], a1[8];
#pragma unroll
  for (int j = 0; j < 8; ++j) { a0[j] = 0.f; a1[j] = 0.f; }
#pragma unroll
  for (int i = 0; i < 16; ++i) {
    const float x0 = vin[0][rl + 8 * i], x1 = vin[1][rl + 8 * i];
#pragma unroll
    for (int j = 0; j < 4; ++j) {
      const float lo = bf_lo(wv[i][j]), hi = bf_hi(wv[i][j]);
      a0[2 * j] = fmaf(x0, lo, a0[2 * j]); a0[2 * j + 1] = fmaf(x0, hi, a0[2 * j + 1]);
      a1[2 * j] = fmaf(x1, lo, a1[2 * j]); a1[2 * j + 1] = fmaf(x1, hi, a1[2 * j + 1]);
    }
  }
#pragma unroll
  for (int j = 0; j < 8; ++j) { red[rl][0][cl * 8 + j] = a0[j]; red[rl][1][cl * 8 + j] = a1[j]; }
  __syncthreads();
  float* fp = reinterpret_cast<float*>(ws + WS_FP);
  {
    float s0 = 0.f, s1 = 0.f;
#pragma unroll
    for (int k = 0; k < 8; ++k) { s0 += red[k][0][tid]; s1 += red[k][1][tid]; }
    __hip_atomic_store(fp + (size_t)(kb * NB + 0) * DM + cgp * 192 + tid, s0, __ATOMIC_RELAXED, __HIP_MEMORY_SCOPE_AGENT);
    __hip_atomic_store(fp + (size_t)(kb * NB + 1) * DM + cgp * 192 + tid, s1, __ATOMIC_RELAXED, __HIP_MEMORY_SCOPE_AGENT);
  }
  asm volatile("s_waitcnt vmcnt(0)" ::: "memory");
  __syncthreads();
  if (tid == 0) {
    unsigned* cnt = reinterpret_cast<unsigned*>(ws + WS_CNT) + cgp * 16;
    const unsigned old = __hip_atomic_fetch_add(cnt, 1u, __ATOMIC_RELAXED, __HIP_MEMORY_SCOPE_AGENT);
    const unsigned last = (old == 67u) ? 1u : 0u;
    if (last) __builtin_amdgcn_fence(__ATOMIC_ACQUIRE, "agent");
    flag = last;
  }
  __syncthreads();
  if (flag == 0u) return;
  __shared__ float gsh[NB];
  {
    const int wave = tid >> 6, lane = tid & 63;
    if (wave < 2) {
      float sum = 0.f;
      for (int k = lane; k < DM; k += 64) sum += rbf(bf2f(x[wave * DM + k]) * bf2f(gw[k]));
      for (int o = 32; o >= 1; o >>= 1) sum += __shfl_xor(sum, o);
      if (lane == 0) gsh[wave] = rbf(1.f / (1.f + __expf(-rbf(sum))));
    }
  }
  __syncthreads();
  const int n = cgp * 192 + tid;
#pragma unroll
  for (int b = 0; b < NB; ++b) {
    float a = 0.f, bb = 0.f;
    for (int k = 0; k < 32; ++k) a += fp[(size_t)(k * NB + b) * DM + n];
    for (int k = 32; k < 68; ++k) bb += fp[(size_t)(k * NB + b) * DM + n];
    const float g = gsh[b];
    const float r1 = rbf(g * rbf(a)), r2 = rbf(rbf(1.f - g) * rbf(bb));
    out[b * DM + n] = f2bf(r1 + r2);
  }
}

extern "C" void kernel_launch(void* const* d_in, const int* in_sizes, int n_in, void* d_out, int out_size, void* d_ws, size_t ws_size,
                              hipStream_t stream) {
  const unsigned short* x = (const unsigned short*)d_in[0];
  const unsigned short* kc = (const unsigned short*)d_in[1];
  const unsigned short* vc = (const unsigned short*)d_in[2];
  const unsigned short* wq = (const unsigned short*)d_in[3];
  const unsigned short* wo = (const unsigned short*)d_in[4];
  const unsigned short* wg = (const unsigned short*)d_in[6];
  const unsigned short* wu = (const unsigned short*)d_in[7];
  const unsigned short* wd = (const unsigned short*)d_in[8];
  const unsigned short* gw = (const unsigned short*)d_in[9];
  unsigned char* ws = (unsigned char*)d_ws;
  proj_kernel<<<dim3(QN / 64 + 2 * (DF / 64)), dim3(256), 0, stream>>>(x, wq, wg, wu, ws);
  attn_kernel<<<dim3(NSPLIT, HKV, NB), dim3(256), 0, stream>>>(kc, vc, ws);
  final_kernel<<<dim3(68, 3), dim3(192), 0, stream>>>(x, wo, wd, gw, ws, (unsigned short*)d_out);
}
```

```cpp
#include <hip/hip_runtime.h>
#include <hip/hip_bf16.h>
#include <cstdint>

constexpr int NB = 2, DM = 576, HQ = 32, HKV = 16, SK = 32768, DH = 128, DF = 4608;
constexpr int QN = HQ * DH;
constexpr int NSPLIT = 16;
constexpr int WAVES = 4, PAGE = 16;
constexpr int TOK_PER_WAVE = SK / NSPLIT / WAVES;
constexpr int PPW = TOK_PER_WAVE / PAGE;
constexpr float SCALE = 0.088388347648318440f;
constexpr float LOG2E = 1.4426950408889634f;

constexpr size_t WS_CNT = 0;
constexpr int KS = 8;
constexpr size_t WS_Q = 4096;
constexpr size_t WS_G = WS_Q + (size_t)KS * NB * QN * 4;
constexpr size_t WS_U = WS_G + (size_t)KS * NB * DF * 4;
constexpr size_t WS_PML = WS_U + (size_t)KS * NB * DF * 4;
constexpr size_t WS_PO = WS_PML + (size_t)NB * HQ * NSPLIT * 2 * 4;
constexpr size_t WS_FP = WS_PO + (size_t)NB * HQ * NSPLIT * DH * 4;

using bf16x2 = __attribute__((ext_vector_type(2))) __bf16;
using u32x4 = __attribute__((ext_vector_type(4))) unsigned;
using f32x4 = __attribute__((ext_vector_type(4))) float;

__device__ __forceinline__ float dot2(unsigned k, unsigned q, float acc) {
  return __builtin_amdgcn_fdot2_f32_bf16(__builtin_bit_cast(bf16x2, k), __builtin_bit_cast(bf16x2, q), acc, false);
}
__device__ __forceinline__ float bf_lo(unsigned w) { return __builtin_bit_cast(float, w << 16); }
__device__ __forceinline__ float bf_hi(unsigned w) { return __builtin_bit_cast(float, w & 0xffff0000u); }
__device__ __forceinline__ float bf2f(unsigned short h) { return __builtin_bit_cast(float, (unsigned)h << 16); }
__device__ __forceinline__ unsigned short f2bf(float f) {
  unsigned u = __builtin_bit_cast(unsigned, f);
  u += 0x7fffu + ((u >> 16) & 1u);
  return (unsigned short)(u >> 16);
}
__device__ __forceinline__ float rbf(float f) { return bf2f(f2bf(f)); }
template <int CTRL> __device__ __forceinline__ float dpp(float x) {
  return __builtin_bit_cast(float, __builtin_amdgcn_mov_dpp(__builtin_bit_cast(int, x), CTRL, 0xf, 0xf, true));
}
constexpr int XOR1 = 0xB1, XOR2 = 0x4E, XOR7 = 0x141, XOR8 = 0x128;
__device__ __forceinline__ float row16_allsum(float x) {
  x += dpp<XOR8>(x); x += dpp<XOR7>(x); x += dpp<XOR2>(x); x += dpp<XOR1>(x); return x;
}

__global__ void __launch_bounds__(256) proj_kernel(const unsigned short* __restrict__ x, const unsigned short* __restrict__ wq,
                                                   const unsigned short* __restrict__ wg, const unsigned short* __restrict__ wu,
                                                   unsigned char* __restrict__ ws) {
  __shared__ float red[4][NB][512];
  const int tid = threadIdx.x, cb = blockIdx.x, ks = blockIdx.y;
  const int wave = __builtin_amdgcn_readfirstlane(tid >> 6), lane = tid & 63;
  if (cb == 0 && ks == 0 && tid < 64) reinterpret_cast<unsigned*>(ws + WS_CNT)[tid * 16] = 0u;
  const unsigned short* w; int ncol, c0; float* dst;
  if (cb < QN / 512) { w = wq; ncol = QN; c0 = cb * 512; dst = reinterpret_cast<float*>(ws + WS_Q); }
  else if (cb < QN / 512 + DF / 512) { w = wg; ncol = DF; c0 = (cb - QN / 512) * 512; dst = reinterpret_cast<float*>(ws + WS_G); }
  else { w = wu; ncol = DF; c0 = (cb - QN / 512 - DF / 512) * 512; dst = reinterpret_cast<float*>(ws + WS_U); }
  const int r0 = ks * 72 + wave * 18;
  u32x4 wv[18];
#pragma unroll
  for (int i = 0; i < 18; ++i) wv[i] = *reinterpret_cast<const u32x4*>(w + (size_t)(r0 + i) * ncol + c0 + lane * 8);
  float a0[8], a1[8];
#pragma unroll
  for (int j = 0; j < 8; ++j) { a0[j] = 0.f; a1[j] = 0.f; }
#pragma unroll
  for (int i = 0; i < 18; ++i) {
    const float x0 = bf2f(x[r0 + i]), x1 = bf2f(x[DM + r0 + i]);
#pragma unroll
    for (int j = 0; j < 4; ++j) {
      const float lo = bf_lo(wv[i][j]), hi = bf_hi(wv[i][j]);
      a0[2 * j] = fmaf(x0, lo, a0[2 * j]); a0[2 * j + 1] = fmaf(x0, hi, a0[2 * j + 1]);
      a1[2 * j] = fmaf(x1, lo, a1[2 * j]); a1[2 * j + 1] = fmaf(x1, hi, a1[2 * j + 1]);
    }
  }
  *reinterpret_cast<f32x4*>(&red[wave][0][lane * 8]) = f32x4{a0[0], a0[1], a0[2], a0[3]};
  *reinterpret_cast<f32x4*>(&red[wave][0][lane * 8 + 4]) = f32x4{a0[4], a0[5], a0[6], a0[7]};
  *reinterpret_cast<f32x4*>(&red[wave][1][lane * 8]) = f32x4{a1[0], a1[1], a1[2], a1[3]};
  *reinterpret_cast<f32x4*>(&red[wave][1][lane * 8 + 4]) = f32x4{a1[4], a1[5], a1[6], a1[7]};
  __syncthreads();
#pragma unroll
  for (int e = tid; e < NB * 512; e += 256) {
    const int b = e >> 9, c = e & 511;
    dst[(size_t)(ks * NB + b) * ncol + c0 + c] = (red[0][b][c] + red[1][b][c]) + (red[2][b][c] + red[3][b][c]);
  }
}

__global__ void __launch_bounds__(256, 2) attn_kernel(const unsigned short* __restrict__ kc, const unsigned short* __restrict__ vc,
                                                      unsigned char* __restrict__ ws) {
  __shared__ __attribute__((aligned(16))) float mrg[16][2][4 + DH];
  const int s = blockIdx.x, kvh = blockIdx.y, b = blockIdx.z;
  const int tid = threadIdx.x, lane = tid & 63, wave = __builtin_amdgcn_readfirstlane(tid >> 6), r = lane >> 4, c = lane & 15;
  constexpr float C2 = SCALE * LOG2E;
  u32x4 q0, q1;
  {
    const float* qp = reinterpret_cast<const float*>(ws + WS_Q) + b * QN + kvh * 2 * DH + 8 * c;
    f32x4 s0 = {0.f, 0.f, 0.f, 0.f}, s1 = s0, s2 = s0, s3 = s0;
#pragma unroll
    for (int k = 0; k < KS; ++k) {
      const float* p = qp + (size_t)k * NB * QN;
      s0 += *reinterpret_cast<const f32x4*>(p); s1 += *reinterpret_cast<const f32x4*>(p + 4);
      s2 += *reinterpret_cast<const f32x4*>(p + DH); s3 += *reinterpret_cast<const f32x4*>(p + DH + 4);
    }
    q0 = u32x4{f2bf(s0[0]) | ((unsigned)f2bf(s0[1]) << 16), f2bf(s0[2]) | ((unsigned)f2bf(s0[3]) << 16), f2bf(s1[0]) | ((unsigned)f2bf(s1[1]) << 16), f2bf(s1[2]) | ((unsigned)f2bf(s1[3]) << 16)};
    q1 = u32x4{f2bf(s2[0]) | ((unsigned)f2bf(s2[1]) << 16), f2bf(s2[2]) | ((unsigned)f2bf(s2[3]) << 16), f2bf(s3[0]) | ((unsigned)f2bf(s3[1]) << 16), f2bf(s3[2]) | ((unsigned)f2bf(s3[3]) << 16)};
  }
  const size_t base = ((size_t)(b * HKV + kvh) * SK + (size_t)s * (SK / NSPLIT) + (size_t)wave * PAGE + r) * DH + 8 * c;
  const u32x4* kp = reinterpret_cast<const u32x4*>(kc + base);
  const u32x4* vp = reinterpret_cast<const u32x4*>(vc + base);
  u32x4 Kn[4], Vn[4];
  const int rot = (s * 2 + kvh * 7 + b * 13);
#pragma unroll
  for (int t = 0; t < 4; ++t) { Kn[t] = __builtin_nontemporal_load(kp + (rot & (PPW - 1)) * (256 * WAVES) + t * 64); Vn[t] = __builtin_nontemporal_load(vp + (rot & (PPW - 1)) * (256 * WAVES) + t * 64); }
  float m0 = -INFINITY, m1 = -INFINITY, l0 = 0.f, l1 = 0.f, acc0[8], acc1[8];
#pragma unroll
  for (int k = 0; k < 8; ++k) { acc0[k] = 0.f; acc1[k] = 0.f; }
  for (int i = 0; i < PPW; ++i) {
    u32x4 Kc[4], Vc[4];
#pragma unroll
    for (int t = 0; t < 4; ++t) { Kc[t] = Kn[t]; Vc[t] = Vn[t]; }
    if (i + 1 < PPW) {
#pragma unroll
      for (int t = 0; t < 4; ++t) { Kn[t] = __builtin_nontemporal_load(kp + ((i + 1 + rot) & (PPW - 1)) * (256 * WAVES) + t * 64); Vn[t] = __builtin_nontemporal_load(vp + ((i + 1 + rot) & (PPW - 1)) * (256 * WAVES) + t * 64); }
    }
    float x0[4], x1[4];
#pragma unroll
    for (int t = 0; t < 4; ++t) {
      float d0 = 0.f, d1 = 0.f;
#pragma unroll
      for (int k = 0; k < 4; ++k) { d0 = dot2(Kc[t][k], q0[k], d0); d1 = dot2(Kc[t][k], q1[k], d1); }
      x0[t] = row16_allsum(d0) * C2; x1[t] = row16_allsum(d1) * C2;
    }
    const float mn0 = fmaxf(fmaxf(fmaxf(x0[0], x0[1]), fmaxf(x0[2], x0[3])), m0);
    const float mn1 = fmaxf(fmaxf(fmaxf(x1[0], x1[1]), fmaxf(x1[2], x1[3])), m1);
    const float al0 = __builtin_amdgcn_exp2f(m0 - mn0), al1 = __builtin_amdgcn_exp2f(m1 - mn1);
    m0 = mn0; m1 = mn1;
    float p0[4], p1[4];
#pragma unroll
    for (int t = 0; t < 4; ++t) { p0[t] = __builtin_amdgcn_exp2f(x0[t] - mn0); p1[t] = __builtin_amdgcn_exp2f(x1[t] - mn1); }
    l0 = fmaf(l0, al0, (p0[0] + p0[1]) + (p0[2] + p0[3]));
    l1 = fmaf(l1, al1, (p1[0] + p1[1]) + (p1[2] + p1[3]));
#pragma unroll
    for (int k = 0; k < 8; ++k) { acc0[k] *= al0; acc1[k] *= al1; }
#pragma unroll
    for (int t = 0; t < 4; ++t) {
#pragma unroll
      for (int k = 0; k < 4; ++k) {
        const float lo = bf_lo(Vc[t][k]), hi = bf_hi(Vc[t][k]);
        acc0[2 * k] = fmaf(p0[t], lo, acc0[2 * k]); acc0[2 * k + 1] = fmaf(p0[t], hi, acc0[2 * k + 1]);
        acc1[2 * k] = fmaf(p1[t], lo, acc1[2 * k]); acc1[2 * k + 1] = fmaf(p1[t], hi, acc1[2 * k + 1]);
      }
    }
  }
  {
    float* rec0 = &mrg[wave * 4 + r][0][0];
    float* rec1 = &mrg[wave * 4 + r][1][0];
    *reinterpret_cast<f32x4*>(rec0 + 4 + 8 * c) = f32x4{acc0[0], acc0[1], acc0[2], acc0[3]};
    *reinterpret_cast<f32x4*>(rec0 + 8 + 8 * c) = f32x4{acc0[4], acc0[5], acc0[6], acc0[7]};
    *reinterpret_cast<f32x4*>(rec1 + 4 + 8 * c) = f32x4{acc1[0], acc1[1], acc1[2], acc1[3]};
    *reinterpret_cast<f32x4*>(rec1 + 8 + 8 * c) = f32x4{acc1[4], acc1[5], acc1[6], acc1[7]};
    if (c == 0) { rec0[0] = m0; rec0[1] = l0; rec1[0] = m1; rec1[1] = l1; }
  }
  __syncthreads();
  {
    const int g = tid >> 7, d = tid & 127;
    float M = -INFINITY;
#pragma unroll
    for (int w = 0; w < 16; ++w) M = fmaxf(M, mrg[w][g][0]);
    float L = 0.f, o = 0.f;
#pragma unroll
    for (int w = 0; w < 16; ++w) { const float wt = __builtin_amdgcn_exp2f(mrg[w][g][0] - M); L = fmaf(wt, mrg[w][g][1], L); o = fmaf(wt, mrg[w][g][4 + d], o); }
    const size_t slot = ((size_t)(b * HQ + kvh * 2 + g)) * NSPLIT + s;
    reinterpret_cast<float*>(ws + WS_PO)[slot * DH + d] = o;
    if (d == 0) { float* pml = reinterpret_cast<float*>(ws + WS_PML) + slot * 2; pml[0] = M; pml[1] = L; }
  }
}

__global__ void __launch_bounds__(192) final_kernel(const unsigned short* __restrict__ x, const unsigned short* __restrict__ wo,
                                                    const unsigned short* __restrict__ wd, const unsigned short* __restrict__ gw,
                                                    unsigned char* __restrict__ ws, unsigned short* __restrict__ out) {
  __shared__ float vin[NB][128];
  __shared__ float red[8][NB][192];
  __shared__ unsigned flag;
  const int kb = blockIdx.x, cgp = blockIdx.y, tid = threadIdx.x;
  const int cl = tid % 24, rl = tid / 24;
  const unsigned short* w = (kb < 32 ? wo + (size_t)kb * 128 * DM : wd + (size_t)(kb - 32) * 128 * DM) + cgp * 192 + cl * 8;
  u32x4 wv[16];
#pragma unroll
  for (int i = 0; i < 16; ++i) wv[i] = *reinterpret_cast<const u32x4*>(w + (size_t)(rl + 8 * i) * DM);
  for (int e = tid; e < NB * 128; e += 192) {
    const int b = e >> 7, d = e & 127;
    float v;
    if (kb < 32) {
      const size_t row = (size_t)(b * HQ + kb) * NSPLIT;
      const float* pml = reinterpret_cast<const float*>(ws + WS_PML) + row * 2;
      const float* po = reinterpret_cast<const float*>(ws + WS_PO) + row * DH + d;
      float M = -INFINITY;
      for (int s = 0; s < NSPLIT; ++s) M = fmaxf(M, pml[2 * s]);
      float L = 0.f, o = 0.f;
      for (int s = 0; s < NSPLIT; ++s) { const float wt = __builtin_amdgcn_exp2f(pml[2 * s] - M); L = fmaf(wt, pml[2 * s + 1], L); o = fmaf(wt, po[(size_t)s * DH], o); }
      v = rbf(o / L);
    } else {
      const int f = (kb - 32) * 128 + d;
      float gs = 0.f, us = 0.f;
#pragma unroll
      for (int k = 0; k < KS; ++k) { gs += reinterpret_cast<const float*>(ws + WS_G)[(size_t)(k * NB + b) * DF + f]; us += reinterpret_cast<const float*>(ws + WS_U)[(size_t)(k * NB + b) * DF + f]; }
      const float g = rbf(gs), u = rbf(us);
      const float t = tanhf(0.7978845608028654f * (g + 0.044715f * g * g * g));
      const float ge = rbf(0.5f * g * (1.f + t));
      v = rbf(ge * u);
    }
    vin[b][d] = v;
  }
  __syncthreads();
  float a0[8], a1[8];
#pragma unroll
  for (int j = 0; j < 8; ++j) { a0[j] = 0.f; a1[j] = 0.f; }
#pragma unroll
  for (int i = 0; i < 16; ++i) {
    const float x0 = vin[0][rl + 8 * i], x1 = vin[1][rl + 8 * i];
#pragma unroll
    for (int j = 0; j < 4; ++j) {
      const float lo = bf_lo(wv[i][j]), hi = bf_hi(wv[i][j]);
      a0[2 * j] = fmaf(x0, lo, a0[2 * j]); a0[2 * j + 1] = fmaf(x0, hi, a0[2 * j + 1]);
      a1[2 * j] = fmaf(x1, lo, a1[2 * j]); a1[2 * j + 1] = fmaf(x1, hi, a1[2 * j + 1]);
    }
  }
#pragma unroll
  for (int j = 0; j < 8; ++j) { red[rl][0][cl * 8 + j] = a0[j]; red[rl][1][cl * 8 + j] = a1[j]; }
  __syncthreads();
  float* fp = reinterpret_cast<float*>(ws + WS_FP);
  {
    float s0 = 0.f, s1 = 0.f;
#pragma unroll
    for (int k = 0; k < 8; ++k) { s0 += red[k][0][tid]; s1 += red[k][1][tid]; }
    __hip_atomic_store(fp + (size_t)(kb * NB + 0) * DM + cgp * 192 + tid, s0, __ATOMIC_RELAXED, __HIP_MEMORY_SCOPE_AGENT);
    __hip_atomic_store(fp + (size_t)(kb * NB + 1) * DM + cgp * 192 + tid, s1, __ATOMIC_RELAXED, __HIP_MEMORY_SCOPE_AGENT);
  }
  asm volatile("s_waitcnt vmcnt(0)" ::: "memory");
  __syncthreads();
  if (tid == 0) {
    unsigned* cnt = reinterpret_cast<unsigned*>(ws + WS_CNT) + cgp * 16;
    const unsigned old = __hip_atomic_fetch_add(cnt, 1u, __ATOMIC_RELAXED, __HIP_MEMORY_SCOPE_AGENT);
    const unsigned last = (old == 67u) ? 1u : 0u;
    if (last) __builtin_amdgcn_fence(__ATOMIC_ACQUIRE, "agent");
    flag = last;
  }
  __syncthreads();
  if (flag == 0u) return;
  __shared__ float gsh[NB];
  {
    const int wave = tid >> 6, lane = tid & 63;
    if (wave < 2) {
      float sum = 0.f;
      for (int k = lane; k < DM; k += 64) sum += rbf(bf2f(x[wave * DM + k]) * bf2f(gw[k]));
      for (int o = 32; o >= 1; o >>= 1) sum += __shfl_xor(sum, o);
      if (lane == 0) gsh[wave] = rbf(1.f / (1.f + __expf(-rbf(sum))));
    }
  }
  __syncthreads();
  const int n = cgp * 192 + tid;
#pragma unroll
  for (int b = 0; b < NB; ++b) {
    float a = 0.f, bb = 0.f;
    for (int k = 0; k < 32; ++k) a += fp[(size_t)(k * NB + b) * DM + n];
    for (int k = 32; k < 68; ++k) bb += fp[(size_t)(k * NB + b) * DM + n];
    const float g = gsh[b];
    const float r1 = rbf(g * rbf(a)), r2 = rbf(rbf(1.f - g) * rbf(bb));
    out[b * DM + n] = f2bf(r1 + r2);
  }
}

extern "C" void kernel_launch(void* const* d_in, const int* in_sizes, int n_in, void* d_out, int out_size, void* d_ws, size_t ws_size,
                              hipStream_t stream) {
  const unsigned short* x = (const unsigned short*)d_in[0];
  const unsigned short* kc = (const unsigned short*)d_in[1];
  const unsigned short* vc = (const unsigned short*)d_in[2];
  const unsigned short* wq = (const unsigned short*)d_in[3];
  const unsigned short* wo = (const unsigned short*)d_in[4];
  const unsigned short* wg = (const unsigned short*)d_in[6];
  const unsigned short* wu = (const unsigned short*)d_in[7];
  const unsigned short* wd = (const unsigned short*)d_in[8];
  const unsigned short* gw = (const unsigned short*)d_in[9];
  unsigned char* ws = (unsigned char*)d_ws;
  proj_kernel<<<dim3(QN / 512 + 2 * (DF / 512), KS), dim3(256), 0, stream>>>(x, wq, wg, wu, ws);
  attn_kernel<<<dim3(NSPLIT, HKV, NB), dim3(256), 0, stream>>>(kc, vc, ws);
  final_kernel<<<dim3(68, 3), dim3(192), 0, stream>>>(x, wo, wd, gw, ws, (unsigned short*)d_out);
}
```

```cpp
#include <hip/hip_runtime.h>
#include <hip/hip_bf16.h>
#include <cstdint>

constexpr int NB = 2, DM = 576, HQ = 32, HKV = 16, SK = 32768, DH = 128, DF = 4608;
constexpr int QN = HQ * DH;
constexpr int NSPLIT = 16;
constexpr int WAVES = 4, PAGE = 16;
constexpr int TOK_PER_WAVE = SK / NSPLIT / WAVES;
constexpr int PPW = TOK_PER_WAVE / PAGE;
constexpr float SCALE = 0.088388347648318440f;
constexpr float LOG2E = 1.4426950408889634f;

constexpr size_t WS_CNT = 0;
constexpr int KS = 8;
constexpr size_t WS_Q = 4096;
constexpr size_t WS_G = WS_Q + (size_t)KS * NB * QN * 4;
constexpr size_t WS_U = WS_G + (size_t)KS * NB * DF * 4;
constexpr size_t WS_PML = WS_U + (size_t)KS * NB * DF * 4;
constexpr size_t WS_PO = WS_PML + (size_t)NB * HQ * NSPLIT * 2 * 4;
constexpr size_t WS_FP = WS_PO + (size_t)NB * HQ * NSPLIT * DH * 4;

using bf16x2 = __attribute__((ext_vector_type(2))) __bf16;
using u32x4 = __attribute__((ext_vector_type(4))) unsigned;
using f32x4 = __attribute__((ext_vector_type(4))) float;

__device__ __forceinline__ float dot2(unsigned k, unsigned q, float acc) {
  return __builtin_amdgcn_fdot2_f32_bf16(__builtin_bit_cast(bf16x2, k), __builtin_bit_cast(bf16x2, q), acc, false);
}
__device__ __forceinline__ float bf_lo(unsigned w) { return __builtin_bit_cast(float, w << 16); }
__device__ __forceinline__ float bf_hi(unsigned w) { return __builtin_bit_cast(float, w & 0xffff0000u); }
__device__ __forceinline__ float bf2f(unsigned short h) { return __builtin_bit_cast(float, (unsigned)h << 16); }
__device__ __forceinline__ unsigned short f2bf(float f) {
  unsigned u = __builtin_bit_cast(unsigned, f);
  u += 0x7fffu + ((u >> 16) & 1u);
  return (unsigned short)(u >> 16);
}
__device__ __forceinline__ float rbf(float f) { return bf2f(f2bf(f)); }
template <int CTRL> __device__ __forceinline__ float dpp(float x) {
  return __builtin_bit_cast(float, __builtin_amdgcn_mov_dpp(__builtin_bit_cast(int, x), CTRL, 0xf, 0xf, true));
}
constexpr int XOR1 = 0xB1, XOR2 = 0x4E, XOR7 = 0x141, XOR8 = 0x128;
__device__ __forceinline__ float row16_allsum(float x) {
  x += dpp<XOR8>(x); x += dpp<XOR7>(x); x += dpp<XOR2>(x); x += dpp<XOR1>(x); return x;
}

__global__ void __launch_bounds__(256) proj_kernel(const unsigned short* __restrict__ x, const unsigned short* __restrict__ wq,
                                                   const unsigned short* __restrict__ wg, const unsigned short* __restrict__ wu,
                                                   unsigned char* __restrict__ ws) {
  __shared__ float red[4][NB][512];
  const int tid = threadIdx.x, cb = blockIdx.x, ks = blockIdx.y;
  const int wave = __builtin_amdgcn_readfirstlane(tid >> 6), lane = tid & 63;
  if (cb == 0 && ks == 0 && tid < 64) reinterpret_cast<unsigned*>(ws + WS_CNT)[tid * 16] = 0u;
  const unsigned short* w; int ncol, c0; float* dst;
  if (cb < QN / 512) { w = wq; ncol = QN; c0 = cb * 512; dst = reinterpret_cast<float*>(ws + WS_Q); }
  else if (cb < QN / 512 + DF / 512) { w = wg; ncol = DF; c0 = (cb - QN / 512) * 512; dst = reinterpret_cast<float*>(ws + WS_G); }
  else { w = wu; ncol = DF; c0 = (cb - QN / 512 - DF / 512) * 512; dst = reinterpret_cast<float*>(ws + WS_U); }
  const int r0 = ks * 72 + wave * 18;
  u32x4 wv[18];
#pragma unroll
  for (int i = 0; i < 18; ++i) wv[i] = *reinterpret_cast<const u32x4*>(w + (size_t)(r0 + i) * ncol + c0 + lane * 8);
  float a0[8], a1[8];
#pragma unroll
  for (int j = 0; j < 8; ++j) { a0[j] = 0.f; a1[j] = 0.f; }
#pragma unroll
  for (int i = 0; i < 18; ++i) {
    const float x0 = bf2f(x[r0 + i]), x1 = bf2f(x[DM + r0 + i]);
#pragma unroll
    for (int j = 0; j < 4; ++j) {
      const float lo = bf_lo(wv[i][j]), hi = bf_hi(wv[i][j]);
      a0[2 * j] = fmaf(x0, lo, a0[2 * j]); a0[2 * j + 1] = fmaf(x0, hi, a0[2 * j + 1]);
      a1[2 * j] = fmaf(x1, lo, a1[2 * j]); a1[2 * j + 1] = fmaf(x1, hi, a1[2 * j + 1]);
    }
  }
  *reinterpret_cast<f32x4*>(&red[wave][0][lane * 8]) = f32x4{a0[0], a0[1], a0[2], a0[3]};
  *reinterpret_cast<f32x4*>(&red[wave][0][lane * 8 + 4]) = f32x4{a0[4], a0[5], a0[6], a0[7]};
  *reinterpret_cast<f32x4*>(&red[wave][1][lane * 8]) = f32x4{a1[0], a1[1], a1[2], a1[3]};
  *reinterpret_cast<f32x4*>(&red[wave][1][lane * 8 + 4]) = f32x4{a1[4], a1[5], a1[6], a1[7]};
  __syncthreads();
#pragma unroll
  for (int e = tid; e < NB * 512; e += 256) {
    const int b = e >> 9, c = e & 511;
    dst[(size_t)(ks * NB + b) * ncol + c0 + c] = (red[0][b][c] + red[1][b][c]) + (red[2][b][c] + red[3][b][c]);
  }
}

constexpr int N_ATT = NB * HKV * NSPLIT;
constexpr int N_FIN = 68 * 3;
__device__ __forceinline__ void st_dev(float* p, float v) { __hip_atomic_store(p, v, __ATOMIC_RELAXED, __HIP_MEMORY_SCOPE_AGENT); }

__global__ void __launch_bounds__(256, 3) main_kernel(const unsigned short* __restrict__ kc, const unsigned short* __restrict__ vc,
                                                      const unsigned short* __restrict__ x, const unsigned short* __restrict__ wo,
                                                      const unsigned short* __restrict__ wd, const unsigned short* __restrict__ gw,
                                                      unsigned char* __restrict__ ws, unsigned short* __restrict__ out) {
  __shared__ __attribute__((aligned(16))) float smem[16 * 2 * (4 + DH)];
  __shared__ unsigned flag;
  const int tid = threadIdx.x;
  unsigned* cnts = reinterpret_cast<unsigned*>(ws + WS_CNT);
  if (blockIdx.x < N_ATT) {
    float (*mrg)[2][4 + DH] = reinterpret_cast<float (*)[2][4 + DH]>(smem);
    const int id = blockIdx.x, s = id & 15, kvh = (id >> 4) & 15, b = id >> 8;
    const int lane = tid & 63, wave = __builtin_amdgcn_readfirstlane(tid >> 6), r = lane >> 4, c = lane & 15;
    constexpr float C2 = SCALE * LOG2E;
    const size_t base = ((size_t)(b * HKV + kvh) * SK + (size_t)s * (SK / NSPLIT) + (size_t)wave * PAGE + r) * DH + 8 * c;
    const u32x4* kp = reinterpret_cast<const u32x4*>(kc + base);
    const u32x4* vp = reinterpret_cast<const u32x4*>(vc + base);
    u32x4 Kn[4], Vn[4];
    const int rot = (s * 2 + kvh * 7 + b * 13);
#pragma unroll
    for (int t = 0; t < 4; ++t) { Kn[t] = __builtin_nontemporal_load(kp + (rot & (PPW - 1)) * (256 * WAVES) + t * 64); Vn[t] = __builtin_nontemporal_load(vp + (rot & (PPW - 1)) * (256 * WAVES) + t * 64); }
    u32x4 q0, q1;
    {
      const float* qp = reinterpret_cast<const float*>(ws + WS_Q) + b * QN + kvh * 2 * DH + 8 * c;
      f32x4 s0 = {0.f, 0.f, 0.f, 0.f}, s1 = s0, s2 = s0, s3 = s0;
#pragma unroll
      for (int k = 0; k < KS; ++k) {
        const float* p = qp + (size_t)k * NB * QN;
        s0 += *reinterpret_cast<const f32x4*>(p); s1 += *reinterpret_cast<const f32x4*>(p + 4);
        s2 += *reinterpret_cast<const f32x4*>(p + DH); s3 += *reinterpret_cast<const f32x4*>(p + DH + 4);
      }
      q0 = u32x4{f2bf(s0[0]) | ((unsigned)f2bf(s0[1]) << 16), f2bf(s0[2]) | ((unsigned)f2bf(s0[3]) << 16), f2bf(s1[0]) | ((unsigned)f2bf(s1[1]) << 16), f2bf(s1[2]) | ((unsigned)f2bf(s1[3]) << 16)};
      q1 = u32x4{f2bf(s2[0]) | ((unsigned)f2bf(s2[1]) << 16), f2bf(s2[2]) | ((unsigned)f2bf(s2[3]) << 16), f2bf(s3[0]) | ((unsigned)f2bf(s3[1]) << 16), f2bf(s3[2]) | ((unsigned)f2bf(s3[3]) << 16)};
    }
    float m0 = -INFINITY, m1 = -INFINITY, l0 = 0.f, l1 = 0.f, acc0[8], acc1[8];
#pragma unroll
    for (int k = 0; k < 8; ++k) { acc0[k] = 0.f; acc1[k] = 0.f; }
    for (int i = 0; i < PPW; ++i) {
      u32x4 Kc[4], Vc[4];
#pragma unroll
      for (int t = 0; t < 4; ++t) { Kc[t] = Kn[t]; Vc[t] = Vn[t]; }
      if (i + 1 < PPW) {
#pragma unroll
        for (int t = 0; t < 4; ++t) { Kn[t] = __builtin_nontemporal_load(kp + ((i + 1 + rot) & (PPW - 1)) * (256 * WAVES) + t * 64); Vn[t] = __builtin_nontemporal_load(vp + ((i + 1 + rot) & (PPW - 1)) * (256 * WAVES) + t * 64); }
      }
      float x0[4], x1[4];
#pragma unroll
      for (int t = 0; t < 4; ++t) {
        float d0 = 0.f, d1 = 0.f;
#pragma unroll
        for (int k = 0; k < 4; ++k) { d0 = dot2(Kc[t][k], q0[k], d0); d1 = dot2(Kc[t][k], q1[k], d1); }
        x0[t] = row16_allsum(d0) * C2; x1[t] = row16_allsum(d1) * C2;
      }
      const float mn0 = fmaxf(fmaxf(fmaxf(x0[0], x0[1]), fmaxf(x0[2], x0[3])), m0);
      const float mn1 = fmaxf(fmaxf(fmaxf(x1[0], x1[1]), fmaxf(x1[2], x1[3])), m1);
      const float al0 = __builtin_amdgcn_exp2f(m0 - mn0), al1 = __builtin_amdgcn_exp2f(m1 - mn1);
      m0 = mn0; m1 = mn1;
      float p0[4], p1[4];
#pragma unroll
      for (int t = 0; t < 4; ++t) { p0[t] = __builtin_amdgcn_exp2f(x0[t] - mn0); p1[t] = __builtin_amdgcn_exp2f(x1[t] - mn1); }
      l0 = fmaf(l0, al0, (p0[0] + p0[1]) + (p0[2] + p0[3]));
      l1 = fmaf(l1, al1, (p1[0] + p1[1]) + (p1[2] + p1[3]));
#pragma unroll
      for (int k = 0; k < 8; ++k) { acc0[k] *= al0; acc1[k] *= al1; }
#pragma unroll
      for (int t = 0; t < 4; ++t) {
#pragma unroll
        for (int k = 0; k < 4; ++k) {
          const float lo = bf_lo(Vc[t][k]), hi = bf_hi(Vc[t][k]);
          acc0[2 * k] = fmaf(p0[t], lo, acc0[2 * k]); acc0[2 * k + 1] = fmaf(p0[t], hi, acc0[2 * k + 1]);
          acc1[2 * k] = fmaf(p1[t], lo, acc1[2 * k]); acc1[2 * k + 1] = fmaf(p1[t], hi, acc1[2 * k + 1]);
        }
      }
    }
    {
      float* rec0 = &mrg[wave * 4 + r][0][0];
      float* rec1 = &mrg[wave * 4 + r][1][0];
      *reinterpret_cast<f32x4*>(rec0 + 4 + 8 * c) = f32x4{acc0[0], acc0[1], acc0[2], acc0[3]};
      *reinterpret_cast<f32x4*>(rec0 + 8 + 8 * c) = f32x4{acc0[4], acc0[5], acc0[6], acc0[7]};
      *reinterpret_cast<f32x4*>(rec1 + 4 + 8 * c) = f32x4{acc1[0], acc1[1], acc1[2], acc1[3]};
      *reinterpret_cast<f32x4*>(rec1 + 8 + 8 * c) = f32x4{acc1[4], acc1[5], acc1[6], acc1[7]};
      if (c == 0) { rec0[0] = m0; rec0[1] = l0; rec1[0] = m1; rec1[1] = l1; }
    }
    __syncthreads();
    {
      const int g = tid >> 7, d = tid & 127;
      float M = -INFINITY;
#pragma unroll
      for (int w = 0; w < 16; ++w) M = fmaxf(M, mrg[w][g][0]);
      float L = 0.f, o = 0.f;
#pragma unroll
      for (int w = 0; w < 16; ++w) { const float wt = __builtin_amdgcn_exp2f(mrg[w][g][0] - M); L = fmaf(wt, mrg[w][g][1], L); o = fmaf(wt, mrg[w][g][4 + d], o); }
      const size_t slot = ((size_t)(b * HQ + kvh * 2 + g)) * NSPLIT + s;
      st_dev(reinterpret_cast<float*>(ws + WS_PO) + slot * DH + d, o);
      if (d == 0) { float* pml = reinterpret_cast<float*>(ws + WS_PML) + slot * 2; st_dev(pml, M); st_dev(pml + 1, L); }
    }
    asm volatile("s_waitcnt vmcnt(0)" ::: "memory");
    __syncthreads();
    if (tid == 0) __hip_atomic_fetch_add(cnts + (3 + kvh) * 16, 1u, __ATOMIC_RELAXED, __HIP_MEMORY_SCOPE_AGENT);
    return;
  }
  float (*vin)[128] = reinterpret_cast<float (*)[128]>(smem);
  float (*red)[NB][192] = reinterpret_cast<float (*)[NB][192]>(smem + 256);
  __shared__ float gsh[NB];
  const int fid = blockIdx.x - N_ATT, kb = fid / 3, cgp = fid % 3;
  const int cl = tid % 24, rl = tid / 24;
  u32x4 wv[16];
  if (tid < 192) {
    const unsigned short* w = (kb < 32 ? wo + (size_t)kb * 128 * DM : wd + (size_t)(kb - 32) * 128 * DM) + cgp * 192 + cl * 8;
#pragma unroll
    for (int i = 0; i < 16; ++i) wv[i] = *reinterpret_cast<const u32x4*>(w + (size_t)(rl + 8 * i) * DM);
  }
  if (kb < 32) {
    if (tid == 0) {
      const unsigned* cnt = cnts + (3 + (kb >> 1)) * 16;
      for (int it = 0; it < (1 << 22); ++it) {
        if (__hip_atomic_load(cnt, __ATOMIC_RELAXED, __HIP_MEMORY_SCOPE_AGENT) >= (unsigned)(NB * NSPLIT)) break;
        __builtin_amdgcn_s_sleep(8);
      }
      __builtin_amdgcn_fence(__ATOMIC_ACQUIRE, "agent");
    }
    __syncthreads();
  }
  {
    const int e = tid, b = e >> 7, d = e & 127;
    float v;
    if (kb < 32) {
      const size_t row = (size_t)(b * HQ + kb) * NSPLIT;
      const float* pml = reinterpret_cast<const float*>(ws + WS_PML) + row * 2;
      const float* po = reinterpret_cast<const float*>(ws + WS_PO) + row * DH + d;
      float M = -INFINITY;
#pragma unroll
      for (int s = 0; s < NSPLIT; ++s) M = fmaxf(M, pml[2 * s]);
      float L = 0.f, o = 0.f;
#pragma unroll
      for (int s = 0; s < NSPLIT; ++s) { const float wt = __builtin_amdgcn_exp2f(pml[2 * s] - M); L = fmaf(wt, pml[2 * s + 1], L); o = fmaf(wt, po[(size_t)s * DH], o); }
      v = rbf(o / L);
    } else {
      const int f = (kb - 32) * 128 + d;
      float gs = 0.f, us = 0.f;
#pragma unroll
      for (int k = 0; k < KS; ++k) { gs += reinterpret_cast<const float*>(ws + WS_G)[(size_t)(k * NB + b) * DF + f]; us += reinterpret_cast<const float*>(ws + WS_U)[(size_t)(k * NB + b) * DF + f]; }
      const float g = rbf(gs), u = rbf(us);
      const float t = tanhf(0.7978845608028654f * (g + 0.044715f * g * g * g));
      const float ge = rbf(0.5f * g * (1.f + t));
      v = rbf(ge * u);
    }
    vin[b][d] = v;
  }
  __syncthreads();
  if (tid < 192) {
    float a0[8], a1[8];
#pragma unroll
    for (int j = 0; j < 8; ++j) { a0[j] = 0.f; a1[j] = 0.f; }
#pragma unroll
    for (int i = 0; i < 16; ++i) {
      const float x0 = vin[0][rl + 8 * i], x1 = vin[1][rl + 8 * i];
#pragma unroll
      for (int j = 0; j < 4; ++j) {
        const float lo = bf_lo(wv[i][j]), hi = bf_hi(wv[i][j]);
        a0[2 * j] = fmaf(x0, lo, a0[2 * j]); a0[2 * j + 1] = fmaf(x0, hi, a0[2 * j + 1]);
        a1[2 * j] = fmaf(x1, lo, a1[2 * j]); a1[2 * j + 1] = fmaf(x1, hi, a1[2 * j + 1]);
      }
    }
#pragma unroll
    for (int j = 0; j < 8; ++j) { red[rl][0][cl * 8 + j] = a0[j]; red[rl][1][cl * 8 + j] = a1[j]; }
  }
  __syncthreads();
  float* fp = reinterpret_cast<float*>(ws + WS_FP);
  if (tid < 192) {
    float s0 = 0.f, s1 = 0.f;
#pragma unroll
    for (int k = 0; k < 8; ++k) { s0 += red[k][0][tid]; s1 += red[k][1][tid]; }
    st_dev(fp + (size_t)(kb * NB + 0) * DM + cgp * 192 + tid, s0);
    st_dev(fp + (size_t)(kb * NB + 1) * DM + cgp * 192 + tid, s1);
  }
  asm volatile("s_waitcnt vmcnt(0)" ::: "memory");
  __syncthreads();
  if (tid == 0) {
    const unsigned old = __hip_atomic_fetch_add(cnts + cgp * 16, 1u, __ATOMIC_RELAXED, __HIP_MEMORY_SCOPE_AGENT);
    const unsigned last = (old == 67u) ? 1u : 0u;
    if (last) __builtin_amdgcn_fence(__ATOMIC_ACQUIRE, "agent");
    flag = last;
  }
  __syncthreads();
  if (flag == 0u) return;
  {
    const int wave = tid >> 6, lane = tid & 63;
    if (wave < 2) {
      float sum = 0.f;
      for (int k = lane; k < DM; k += 64) sum += rbf(bf2f(x[wave * DM + k]) * bf2f(gw[k]));
      for (int o = 32; o >= 1; o >>= 1) sum += __shfl_xor(sum, o);
      if (lane == 0) gsh[wave] = rbf(1.f / (1.f + __expf(-rbf(sum))));
    }
  }
  __syncthreads();
  if (tid < 192) {
    const int n = cgp * 192 + tid;
#pragma unroll
    for (int b = 0; b < NB; ++b) {
      float a = 0.f, bb = 0.f;
#pragma unroll
      for (int k = 0; k < 32; ++k) a += fp[(size_t)(k * NB + b) * DM + n];
#pragma unroll
      for (int k = 32; k < 68; ++k) bb += fp[(size_t)(k * NB + b) * DM + n];
      const float g = gsh[b];
      const float r1 = rbf(g * rbf(a)), r2 = rbf(rbf(1.f - g) * rbf(bb));
      out[b * DM + n] = f2bf(r1 + r2);
    }
  }
}

extern "C" void kernel_launch(void* const* d_in, const int* in_sizes, int n_in, void* d_out, int out_size, void* d_ws, size_t ws_size,
                              hipStream_t stream) {
  const unsigned short* x = (const unsigned short*)d_in[0];
  const unsigned short* kc = (const unsigned short*)d_in[1];
  const unsigned short* vc = (const unsigned short*)d_in[2];
  const unsigned short* wq = (const unsigned short*)d_in[3];
  const unsigned short* wo = (const unsigned short*)d_in[4];
  const unsigned short* wg = (const unsigned short*)d_in[6];
  const unsigned short* wu = (const unsigned short*)d_in[7];
  const unsigned short* wd = (const unsigned short*)d_in[8];
  const unsigned short* gw = (const unsigned short*)d_in[9];
  unsigned char* ws = (unsigned char*)d_ws;
  proj_kernel<<<dim3(QN / 512 + 2 * (DF / 512), KS), dim3(256), 0, stream>>>(x, wq, wg, wu, ws);
  main_kernel<<<dim3(N_ATT + N_FIN), dim3(256), 0, stream>>>(kc, vc, x, wo, wd, gw, ws, (unsigned short*)d_out);
}
```

```cpp
#include <hip/hip_runtime.h>
#include <hip/hip_bf16.h>
#include <cstdint>

constexpr int NB = 2, DM = 576, HQ = 32, HKV = 16, SK = 32768, DH = 128, DF = 4608;
constexpr int QN = HQ * DH;
constexpr int NSPLIT = 16;
constexpr int WAVES = 4, PAGE = 16;
constexpr int TOK_PER_WAVE = SK / NSPLIT / WAVES;
constexpr int PPW = TOK_PER_WAVE / PAGE;
constexpr float SCALE = 0.088388347648318440f;
constexpr float LOG2E = 1.4426950408889634f;

constexpr size_t WS_CNT = 0;
constexpr int KS = 8;
constexpr size_t WS_Q = 4096;
constexpr size_t WS_G = WS_Q + (size_t)KS * NB * QN * 4;
constexpr size_t WS_U = WS_G + (size_t)KS * NB * DF * 4;
constexpr size_t WS_PML = WS_U + (size_t)KS * NB * DF * 4;
constexpr size_t WS_PO = WS_PML + (size_t)NB * HQ * NSPLIT * 2 * 4;
constexpr size_t WS_FP = WS_PO + (size_t)NB * HQ * NSPLIT * DH * 4;

using bf16x2 = __attribute__((ext_vector_type(2))) __bf16;
using u32x4 = __attribute__((ext_vector_type(4))) unsigned;
using f32x4 = __attribute__((ext_vector_type(4))) float;

__device__ __forceinline__ float dot2(unsigned k, unsigned q, float acc) {
  return __builtin_amdgcn_fdot2_f32_bf16(__builtin_bit_cast(bf16x2, k), __builtin_bit_cast(bf16x2, q), acc, false);
}
__device__ __forceinline__ float bf_lo(unsigned w) { return __builtin_bit_cast(float, w << 16); }
__device__ __forceinline__ float bf_hi(unsigned w) { return __builtin_bit_cast(float, w & 0xffff0000u); }
__device__ __forceinline__ float bf2f(unsigned short h) { return __builtin_bit_cast(float, (unsigned)h << 16); }
__device__ __forceinline__ unsigned short f2bf(float f) {
  unsigned u = __builtin_bit_cast(unsigned, f);
  u += 0x7fffu + ((u >> 16) & 1u);
  return (unsigned short)(u >> 16);
}
__device__ __forceinline__ float rbf(float f) { return bf2f(f2bf(f)); }
template <int CTRL> __device__ __forceinline__ float dpp(float x) {
  return __builtin_bit_cast(float, __builtin_amdgcn_mov_dpp(__builtin_bit_cast(int, x), CTRL, 0xf, 0xf, true));
}
constexpr int XOR1 = 0xB1, XOR2 = 0x4E, XOR7 = 0x141, XOR8 = 0x128;
__device__ __forceinline__ float row16_allsum(float x) {
  x += dpp<XOR8>(x); x += dpp<XOR7>(x); x += dpp<XOR2>(x); x += dpp<XOR1>(x); return x;
}

__global__ void __launch_bounds__(256) proj_kernel(const unsigned short* __restrict__ x, const unsigned short* __restrict__ wq,
                                                   const unsigned short* __restrict__ wg, const unsigned short* __restrict__ wu,
                                                   unsigned char* __restrict__ ws) {
  __shared__ float red[4][NB][512];
  const int tid = threadIdx.x, cb = blockIdx.x, ks = blockIdx.y;
  const int wave = __builtin_amdgcn_readfirstlane(tid >> 6), lane = tid & 63;
  if (cb == 0 && ks == 0 && tid < 64) reinterpret_cast<unsigned*>(ws + WS_CNT)[tid * 16] = 0u;
  const unsigned short* w; int ncol, c0; float* dst;
  if (cb < QN / 512) { w = wq; ncol = QN; c0 = cb * 512; dst = reinterpret_cast<float*>(ws + WS_Q); }
  else if (cb < QN / 512 + DF / 512) { w = wg; ncol = DF; c0 = (cb - QN / 512) * 512; dst = reinterpret_cast<float*>(ws + WS_G); }
  else { w = wu; ncol = DF; c0 = (cb - QN / 512 - DF / 512) * 512; dst = reinterpret_cast<float*>(ws + WS_U); }
  const int r0 = ks * 72 + wave * 18;
  u32x4 wv[18];
#pragma unroll
  for (int i = 0; i < 18; ++i) wv[i] = *reinterpret_cast<const u32x4*>(w + (size_t)(r0 + i) * ncol + c0 + lane * 8);
  float a0[8], a1[8];
#pragma unroll
  for (int j = 0; j < 8; ++j) { a0[j] = 0.f; a1[j] = 0.f; }
#pragma unroll
  for (int i = 0; i < 18; ++i) {
    const float x0 = bf2f(x[r0 + i]), x1 = bf2f(x[DM + r0 + i]);
#pragma unroll
    for (int j = 0; j < 4; ++j) {
      const float lo = bf_lo(wv[i][j]), hi = bf_hi(wv[i][j]);
      a0[2 * j] = fmaf(x0, lo, a0[2 * j]); a0[2 * j + 1] = fmaf(x0, hi, a0[2 * j + 1]);
      a1[2 * j] = fmaf(x1, lo, a1[2 * j]); a1[2 * j + 1] = fmaf(x1, hi, a1[2 * j + 1]);
    }
  }
  *reinterpret_cast<f32x4*>(&red[wave][0][lane * 8]) = f32x4{a0[0], a0[1], a0[2], a0[3]};
  *reinterpret_cast<f32x4*>(&red[wave][0][lane * 8 + 4]) = f32x4{a0[4], a0[5], a0[6], a0[7]};
  *reinterpret_cast<f32x4*>(&red[wave][1][lane * 8]) = f32x4{a1[0], a1[1], a1[2], a1[3]};
  *reinterpret_cast<f32x4*>(&red[wave][1][lane * 8 + 4]) = f32x4{a1[4], a1[5], a1[6], a1[7]};
  __syncthreads();
#pragma unroll
  for (int e = tid; e < NB * 512; e += 256) {
    const int b = e >> 9, c = e & 511;
    dst[(size_t)(ks * NB + b) * ncol + c0 + c] = (red[0][b][c] + red[1][b][c]) + (red[2][b][c] + red[3][b][c]);
  }
}

constexpr int N_ATT = NB * HKV * NSPLIT;
constexpr int N_FIN = 68 * 3;
__device__ __forceinline__ void st_dev(float* p, float v) { __hip_atomic_store(p, v, __ATOMIC_RELAXED, __HIP_MEMORY_SCOPE_AGENT); }

__global__ void __launch_bounds__(256, 3) main_kernel(const unsigned short* __restrict__ kc, const unsigned short* __restrict__ vc,
                                                      const unsigned short* __restrict__ x, const unsigned short* __restrict__ wo,
                                                      const unsigned short* __restrict__ wd, const unsigned short* __restrict__ gw,
                                                      unsigned char* __restrict__ ws, unsigned short* __restrict__ out) {
  __shared__ __attribute__((aligned(16))) float smem[16384];
  __shared__ unsigned flag;
  const int tid = threadIdx.x;
  unsigned* cnts = reinterpret_cast<unsigned*>(ws + WS_CNT);
  if (blockIdx.x < N_ATT) {
    float (*mrg)[2][4 + DH] = reinterpret_cast<float (*)[2][4 + DH]>(smem);
    const int id = blockIdx.x, s = id & 15, kvh = (id >> 4) & 15, b = id >> 8;
    const int lane = tid & 63, wave = __builtin_amdgcn_readfirstlane(tid >> 6), r = lane >> 4, c = lane & 15;
    constexpr float C2 = SCALE * LOG2E;
    const size_t base = ((size_t)(b * HKV + kvh) * SK + (size_t)s * (SK / NSPLIT) + (size_t)wave * PAGE + r) * DH + 8 * c;
    const u32x4* kp = reinterpret_cast<const u32x4*>(kc + base);
    const u32x4* vp = reinterpret_cast<const u32x4*>(vc + base);
    u32x4 Kn[4], Vn[4];
    const int rot = (s * 2 + kvh * 7 + b * 13);
#pragma unroll
    for (int t = 0; t < 4; ++t) { Kn[t] = __builtin_nontemporal_load(kp + (rot & (PPW - 1)) * (256 * WAVES) + t * 64); Vn[t] = __builtin_nontemporal_load(vp + (rot & (PPW - 1)) * (256 * WAVES) + t * 64); }
    u32x4 q0, q1;
    {
      const float* qp = reinterpret_cast<const float*>(ws + WS_Q) + b * QN + kvh * 2 * DH + 8 * c;
      f32x4 s0 = {0.f, 0.f, 0.f, 0.f}, s1 = s0, s2 = s0, s3 = s0;
#pragma unroll
      for (int k = 0; k < KS; ++k) {
        const float* p = qp + (size_t)k * NB * QN;
        s0 += *reinterpret_cast<const f32x4*>(p); s1 += *reinterpret_cast<const f32x4*>(p + 4);
        s2 += *reinterpret_cast<const f32x4*>(p + DH); s3 += *reinterpret_cast<const f32x4*>(p + DH + 4);
      }
      q0 = u32x4{f2bf(s0[0]) | ((unsigned)f2bf(s0[1]) << 16), f2bf(s0[2]) | ((unsigned)f2bf(s0[3]) << 16), f2bf(s1[0]) | ((unsigned)f2bf(s1[1]) << 16), f2bf(s1[2]) | ((unsigned)f2bf(s1[3]) << 16)};
      q1 = u32x4{f2bf(s2[0]) | ((unsigned)f2bf(s2[1]) << 16), f2bf(s2[2]) | ((unsigned)f2bf(s2[3]) << 16), f2bf(s3[0]) | ((unsigned)f2bf(s3[1]) << 16), f2bf(s3[2]) | ((unsigned)f2bf(s3[3]) << 16)};
    }
    float m0 = -INFINITY, m1 = -INFINITY, l0 = 0.f, l1 = 0.f, acc0[8], acc1[8];
#pragma unroll
    for (int k = 0; k < 8; ++k) { acc0[k] = 0.f; acc1[k] = 0.f; }
    for (int i = 0; i < PPW; ++i) {
      u32x4 Kc[4], Vc[4];
#pragma unroll
      for (int t = 0; t < 4; ++t) { Kc[t] = Kn[t]; Vc[t] = Vn[t]; }
      if (i + 1 < PPW) {
#pragma unroll
        for (int t = 0; t < 4; ++t) { Kn[t] = __builtin_nontemporal_load(kp + ((i + 1 + rot) & (PPW - 1)) * (256 * WAVES) + t * 64); Vn[t] = __builtin_nontemporal_load(vp + ((i + 1 + rot) & (PPW - 1)) * (256 * WAVES) + t * 64); }
      }
      float x0[4], x1[4];
#pragma unroll
      for (int t = 0; t < 4; ++t) {
        float d0 = 0.f, d1 = 0.f;
#pragma unroll
        for (int k = 0; k < 4; ++k) { d0 = dot2(Kc[t][k], q0[k], d0); d1 = dot2(Kc[t][k], q1[k], d1); }
        x0[t] = row16_allsum(d0) * C2; x1[t] = row16_allsum(d1) * C2;
      }
      const float mn0 = fmaxf(fmaxf(fmaxf(x0[0], x0[1]), fmaxf(x0[2], x0[3])), m0);
      const float mn1 = fmaxf(fmaxf(fmaxf(x1[0], x1[1]), fmaxf(x1[2], x1[3])), m1);
      const float al0 = __builtin_amdgcn_exp2f(m0 - mn0), al1 = __builtin_amdgcn_exp2f(m1 - mn1);
      m0 = mn0; m1 = mn1;
      float p0[4], p1[4];
#pragma unroll
      for (int t = 0; t < 4; ++t) { p0[t] = __builtin_amdgcn_exp2f(x0[t] - mn0); p1[t] = __builtin_amdgcn_exp2f(x1[t] - mn1); }
      l0 = fmaf(l0, al0, (p0[0] + p0[1]) + (p0[2] + p0[3]));
      l1 = fmaf(l1, al1, (p1[0] + p1[1]) + (p1[2] + p1[3]));
#pragma unroll
      for (int k = 0; k < 8; ++k) { acc0[k] *= al0; acc1[k] *= al1; }
#pragma unroll
      for (int t = 0; t < 4; ++t) {
#pragma unroll
        for (int k = 0; k < 4; ++k) {
          const float lo = bf_lo(Vc[t][k]), hi = bf_hi(Vc[t][k]);
          acc0[2 * k] = fmaf(p0[t], lo, acc0[2 * k]); acc0[2 * k + 1] = fmaf(p0[t], hi, acc0[2 * k + 1]);
          acc1[2 * k] = fmaf(p1[t], lo, acc1[2 * k]); acc1[2 * k + 1] = fmaf(p1[t], hi, acc1[2 * k + 1]);
        }
      }
    }
    {
      float* rec0 = &mrg[wave * 4 + r][0][0];
      float* rec1 = &mrg[wave * 4 + r][1][0];
      *reinterpret_cast<f32x4*>(rec0 + 4 + 8 * c) = f32x4{acc0[0], acc0[1], acc0[2], acc0[3]};
      *reinterpret_cast<f32x4*>(rec0 + 8 + 8 * c) = f32x4{acc0[4], acc0[5], acc0[6], acc0[7]};
      *reinterpret_cast<f32x4*>(rec1 + 4 + 8 * c) = f32x4{acc1[0], acc1[1], acc1[2], acc1[3]};
      *reinterpret_cast<f32x4*>(rec1 + 8 + 8 * c) = f32x4{acc1[4], acc1[5], acc1[6], acc1[7]};
      if (c == 0) { rec0[0] = m0; rec0[1] = l0; rec1[0] = m1; rec1[1] = l1; }
    }
    __syncthreads();
    {
      const int g = tid >> 7, d = tid & 127;
      float M = -INFINITY;
#pragma unroll
      for (int w = 0; w < 16; ++w) M = fmaxf(M, mrg[w][g][0]);
      float L = 0.f, o = 0.f;
#pragma unroll
      for (int w = 0; w < 16; ++w) { const float wt = __builtin_amdgcn_exp2f(mrg[w][g][0] - M); L = fmaf(wt, mrg[w][g][1], L); o = fmaf(wt, mrg[w][g][4 + d], o); }
      const size_t slot = ((size_t)(b * HQ + kvh * 2 + g)) * NSPLIT + s;
      st_dev(reinterpret_cast<float*>(ws + WS_PO) + slot * DH + d, o);
      if (d == 0) { float* pml = reinterpret_cast<float*>(ws + WS_PML) + slot * 2; st_dev(pml, M); st_dev(pml + 1, L); }
    }
    asm volatile("s_waitcnt vmcnt(0)" ::: "memory");
    __syncthreads();
    if (tid == 0) __hip_atomic_fetch_add(cnts + (3 + kvh) * 16, 1u, __ATOMIC_RELAXED, __HIP_MEMORY_SCOPE_AGENT);
    return;
  }
  float (*vin)[128] = reinterpret_cast<float (*)[128]>(smem);
  float (*red)[NB][192] = reinterpret_cast<float (*)[NB][192]>(smem + 256);
  __shared__ float gsh[NB];
  const int fid = blockIdx.x - N_ATT, kb = fid / 3, cgp = fid % 3;
  const int cl = tid % 24, rl = tid / 24;
  u32x4 wv[16];
  if (tid < 192) {
    const unsigned short* w = (kb < 32 ? wo + (size_t)kb * 128 * DM : wd + (size_t)(kb - 32) * 128 * DM) + cgp * 192 + cl * 8;
#pragma unroll
    for (int i = 0; i < 16; ++i) wv[i] = *reinterpret_cast<const u32x4*>(w + (size_t)(rl + 8 * i) * DM);
  }
  {
    const int wave = tid >> 6, lane = tid & 63;
    if (wave >= 2) {
      float sum = 0.f;
#pragma unroll
      for (int k = 0; k < DM / 64; ++k) sum += rbf(bf2f(x[(wave - 2) * DM + k * 64 + lane]) * bf2f(gw[k * 64 + lane]));
      for (int o = 32; o >= 1; o >>= 1) sum += __shfl_xor(sum, o);
      if (lane == 0) gsh[wave - 2] = rbf(1.f / (1.f + __expf(-rbf(sum))));
    }
  }
  if (kb < 32) {
    if (tid == 0) {
      const unsigned* cnt = cnts + (3 + (kb >> 1)) * 16;
      for (int it = 0; it < (1 << 22); ++it) {
        if (__hip_atomic_load(cnt, __ATOMIC_RELAXED, __HIP_MEMORY_SCOPE_AGENT) >= (unsigned)(NB * NSPLIT)) break;
        __builtin_amdgcn_s_sleep(8);
      }
      __builtin_amdgcn_fence(__ATOMIC_ACQUIRE, "agent");
    }
    __syncthreads();
  }
  {
    const int e = tid, b = e >> 7, d = e & 127;
    float v;
    if (kb < 32) {
      const size_t row = (size_t)(b * HQ + kb) * NSPLIT;
      const float* pml = reinterpret_cast<const float*>(ws + WS_PML) + row * 2;
      const float* po = reinterpret_cast<const float*>(ws + WS_PO) + row * DH + d;
      float M = -INFINITY;
#pragma unroll
      for (int s = 0; s < NSPLIT; ++s) M = fmaxf(M, pml[2 * s]);
      float L = 0.f, o = 0.f;
#pragma unroll
      for (int s = 0; s < NSPLIT; ++s) { const float wt = __builtin_amdgcn_exp2f(pml[2 * s] - M); L = fmaf(wt, pml[2 * s + 1], L); o = fmaf(wt, po[(size_t)s * DH], o); }
      v = rbf(o / L);
    } else {
      const int f = (kb - 32) * 128 + d;
      float gs = 0.f, us = 0.f;
#pragma unroll
      for (int k = 0; k < KS; ++k) { gs += reinterpret_cast<const float*>(ws + WS_G)[(size_t)(k * NB + b) * DF + f]; us += reinterpret_cast<const float*>(ws + WS_U)[(size_t)(k * NB + b) * DF + f]; }
      const float g = rbf(gs), u = rbf(us);
      const float t = tanhf(0.7978845608028654f * (g + 0.044715f * g * g * g));
      const float ge = rbf(0.5f * g * (1.f + t));
      v = rbf(ge * u);
    }
    vin[b][d] = v;
  }
  __syncthreads();
  if (tid < 192) {
    float a0[8], a1[8];
#pragma unroll
    for (int j = 0; j < 8; ++j) { a0[j] = 0.f; a1[j] = 0.f; }
#pragma unroll
    for (int i = 0; i < 16; ++i) {
      const float x0 = vin[0][rl + 8 * i], x1 = vin[1][rl + 8 * i];
#pragma unroll
      for (int j = 0; j < 4; ++j) {
        const float lo = bf_lo(wv[i][j]), hi = bf_hi(wv[i][j]);
        a0[2 * j] = fmaf(x0, lo, a0[2 * j]); a0[2 * j + 1] = fmaf(x0, hi, a0[2 * j + 1]);
        a1[2 * j] = fmaf(x1, lo, a1[2 * j]); a1[2 * j + 1] = fmaf(x1, hi, a1[2 * j + 1]);
      }
    }
#pragma unroll
    for (int j = 0; j < 8; ++j) { red[rl][0][cl * 8 + j] = a0[j]; red[rl][1][cl * 8 + j] = a1[j]; }
  }
  __syncthreads();
  float* fp = reinterpret_cast<float*>(ws + WS_FP);
  if (tid < 192) {
    float s0 = 0.f, s1 = 0.f;
#pragma unroll
    for (int k = 0; k < 8; ++k) { s0 += red[k][0][tid]; s1 += red[k][1][tid]; }
    st_dev(fp + (size_t)(kb * NB + 0) * DM + cgp * 192 + tid, s0);
    st_dev(fp + (size_t)(kb * NB + 1) * DM + cgp * 192 + tid, s1);
  }
  asm volatile("s_waitcnt vmcnt(0)" ::: "memory");
  __syncthreads();
  if (tid == 0) {
    const unsigned old = __hip_atomic_fetch_add(cnts + cgp * 16, 1u, __ATOMIC_RELAXED, __HIP_MEMORY_SCOPE_AGENT);
    const unsigned last = (old == 67u) ? 1u : 0u;
    if (last) __builtin_amdgcn_fence(__ATOMIC_ACQUIRE, "agent");
    flag = last;
  }
  __syncthreads();
  if (flag == 0u) return;
  for (int e = tid; e < NB * 192; e += 256) {
    const int b = e / 192, n = cgp * 192 + e % 192;
    float v[68];
#pragma unroll
    for (int k = 0; k < 68; ++k) v[k] = fp[(size_t)(k * NB + b) * DM + n];
    float a = 0.f, bb = 0.f;
#pragma unroll
    for (int k = 0; k < 32; ++k) a += v[k];
#pragma unroll
    for (int k = 32; k < 68; ++k) bb += v[k];
    const float g = gsh[b];
    const float r1 = rbf(g * rbf(a)), r2 = rbf(rbf(1.f - g) * rbf(bb));
    out[b * DM + n] = f2bf(r1 + r2);
  }
}

extern "C" void kernel_launch(void* const* d_in, const int* in_sizes, int n_in, void* d_out, int out_size, void* d_ws, size_t ws_size,
                              hipStream_t stream) {
  const unsigned short* x = (const unsigned short*)d_in[0];
  const unsigned short* kc = (const unsigned short*)d_in[1];
  const unsigned short* vc = (const unsigned short*)d_in[2];
  const unsigned short* wq = (const unsigned short*)d_in[3];
  const unsigned short* wo = (const unsigned short*)d_in[4];
  const unsigned short* wg = (const unsigned short*)d_in[6];
  const unsigned short* wu = (const unsigned short*)d_in[7];
  const unsigned short* wd = (const unsigned short*)d_in[8];
  const unsigned short* gw = (const unsigned short*)d_in[9];
  unsigned char* ws = (unsigned char*)d_ws;
  proj_kernel<<<dim3(QN / 512 + 2 * (DF / 512), KS), dim3(256), 0, stream>>>(x, wq, wg, wu, ws);
  main_kernel<<<dim3(N_ATT + N_FIN), dim3(256), 0, stream>>>(kc, vc, x, wo, wd, gw, ws, (unsigned short*)d_out);
}
```

```cpp
#include <hip/hip_runtime.h>
#include <hip/hip_bf16.h>
#include <cstdint>

constexpr int NB = 2, DM = 576, HQ = 32, HKV = 16, SK = 32768, DH = 128, DF = 4608;
constexpr int QN = HQ * DH;
constexpr int NSPLIT = 8;
constexpr int WAVES = 8, PAGE = 16;
constexpr int TOK_PER_WAVE = SK / NSPLIT / WAVES;
constexpr int PPW = TOK_PER_WAVE / PAGE;
constexpr float SCALE = 0.088388347648318440f;
constexpr float LOG2E = 1.4426950408889634f;

constexpr size_t WS_CNT = 0;
constexpr int KS = 8;
constexpr size_t WS_Q = 4096;
constexpr size_t WS_G = WS_Q + (size_t)KS * NB * QN * 4;
constexpr size_t WS_U = WS_G + (size_t)KS * NB * DF * 4;
constexpr size_t WS_PML = WS_U + (size_t)KS * NB * DF * 4;
constexpr size_t WS_PO = WS_PML + (size_t)NB * HQ * NSPLIT * 2 * 4;
constexpr size_t WS_FP = WS_PO + (size_t)NB * HQ * NSPLIT * DH * 4;

using bf16x2 = __attribute__((ext_vector_type(2))) __bf16;
using u32x4 = __attribute__((ext_vector_type(4))) unsigned;
using f32x4 = __attribute__((ext_vector_type(4))) float;

__device__ __forceinline__ float dot2(unsigned k, unsigned q, float acc) {
  return __builtin_amdgcn_fdot2_f32_bf16(__builtin_bit_cast(bf16x2, k), __builtin_bit_cast(bf16x2, q), acc, false);
}
__device__ __forceinline__ float bf_lo(unsigned w) { return __builtin_bit_cast(float, w << 16); }
__device__ __forceinline__ float bf_hi(unsigned w) { return __builtin_bit_cast(float, w & 0xffff0000u); }
__device__ __forceinline__ float bf2f(unsigned short h) { return __builtin_bit_cast(float, (unsigned)h << 16); }
__device__ __forceinline__ unsigned short f2bf(float f) {
  unsigned u = __builtin_bit_cast(unsigned, f);
  u += 0x7fffu + ((u >> 16) & 1u);
  return (unsigned short)(u >> 16);
}
__device__ __forceinline__ float rbf(float f) { return bf2f(f2bf(f)); }
template <int CTRL> __device__ __forceinline__ float dpp(float x) {
  return __builtin_bit_cast(float, __builtin_amdgcn_mov_dpp(__builtin_bit_cast(int, x), CTRL, 0xf, 0xf, true));
}
constexpr int XOR1 = 0xB1, XOR2 = 0x4E, XOR7 = 0x141, XOR8 = 0x128;
__device__ __forceinline__ float row16_allsum(float x) {
  x += dpp<XOR8>(x); x += dpp<XOR7>(x); x += dpp<XOR2>(x); x += dpp<XOR1>(x); return x;
}

__global__ void __launch_bounds__(256) proj_kernel(const unsigned short* __restrict__ x, const unsigned short* __restrict__ wq,
                                                   const unsigned short* __restrict__ wg, const unsigned short* __restrict__ wu,
                                                   unsigned char* __restrict__ ws) {
  __shared__ float red[4][NB][512];
  const int tid = threadIdx.x, cb = blockIdx.x, ks = blockIdx.y;
  const int wave = __builtin_amdgcn_readfirstlane(tid >> 6), lane = tid & 63;
  if (cb == 0 && ks == 0 && tid < 64) reinterpret_cast<unsigned*>(ws + WS_CNT)[tid * 16] = 0u;
  const unsigned short* w; int ncol, c0; float* dst;
  if (cb < QN / 512) { w = wq; ncol = QN; c0 = cb * 512; dst = reinterpret_cast<float*>(ws + WS_Q); }
  else if (cb < QN / 512 + DF / 512) { w = wg; ncol = DF; c0 = (cb - QN / 512) * 512; dst = reinterpret_cast<float*>(ws + WS_G); }
  else { w = wu; ncol = DF; c0 = (cb - QN / 512 - DF / 512) * 512; dst = reinterpret_cast<float*>(ws + WS_U); }
  const int r0 = ks * 72 + wave * 18;
  u32x4 wv[18];
#pragma unroll
  for (int i = 0; i < 18; ++i) wv[i] = *reinterpret_cast<const u32x4*>(w + (size_t)(r0 + i) * ncol + c0 + lane * 8);
  float a0[8], a1[8];
#pragma unroll
  for (int j = 0; j < 8; ++j) { a0[j] = 0.f; a1[j] = 0.f; }
#pragma unroll
  for (int i = 0; i < 18; ++i) {
    const float x0 = bf2f(x[r0 + i]), x1 = bf2f(x[DM + r0 + i]);
#pragma unroll
    for (int j = 0; j < 4; ++j) {
      const float lo = bf_lo(wv[i][j]), hi = bf_hi(wv[i][j]);
      a0[2 * j] = fmaf(x0, lo, a0[2 * j]); a0[2 * j + 1] = fmaf(x0, hi, a0[2 * j + 1]);
      a1[2 * j] = fmaf(x1, lo, a1[2 * j]); a1[2 * j + 1] = fmaf(x1, hi, a1[2 * j + 1]);
    }
  }
  *reinterpret_cast<f32x4*>(&red[wave][0][lane * 8]) = f32x4{a0[0], a0[1], a0[2], a0[3]};
  *reinterpret_cast<f32x4*>(&red[wave][0][lane * 8 + 4]) = f32x4{a0[4], a0[5], a0[6], a0[7]};
  *reinterpret_cast<f32x4*>(&red[wave][1][lane * 8]) = f32x4{a1[0], a1[1], a1[2], a1[3]};
  *reinterpret_cast<f32x4*>(&red[wave][1][lane * 8 + 4]) = f32x4{a1[4], a1[5], a1[6], a1[7]};
  __syncthreads();
#pragma unroll
  for (int e = tid; e < NB * 512; e += 256) {
    const int b = e >> 9, c = e & 511;
    dst[(size_t)(ks * NB + b) * ncol + c0 + c] = (red[0][b][c] + red[1][b][c]) + (red[2][b][c] + red[3][b][c]);
  }
}

constexpr int N_ATT = NB * HKV * NSPLIT;
constexpr int N_FIN = 68 * 3;
__device__ __forceinline__ void st_dev(float* p, float v) { __hip_atomic_store(p, v, __ATOMIC_RELAXED, __HIP_MEMORY_SCOPE_AGENT); }

__global__ void __launch_bounds__(512, 2) main_kernel(const unsigned short* __restrict__ kc, const unsigned short* __restrict__ vc,
                                                      const unsigned short* __restrict__ x, const unsigned short* __restrict__ wo,
                                                      const unsigned short* __restrict__ wd, const unsigned short* __restrict__ gw,
                                                      unsigned char* __restrict__ ws, unsigned short* __restrict__ out) {
  __shared__ __attribute__((aligned(16))) float smem[16384];
  __shared__ unsigned flag;
  const int tid = threadIdx.x;
  unsigned* cnts = reinterpret_cast<unsigned*>(ws + WS_CNT);
  if (blockIdx.x < N_ATT) {
    float (*mrg)[2][4 + DH] = reinterpret_cast<float (*)[2][4 + DH]>(smem);
    const int id = blockIdx.x, s = id % NSPLIT, kvh = (id / NSPLIT) & 15, b = id / (NSPLIT * 16);
    const int lane = tid & 63, wave = __builtin_amdgcn_readfirstlane(tid >> 6), r = lane >> 4, c = lane & 15;
    constexpr float C2 = SCALE * LOG2E;
    const size_t base = ((size_t)(b * HKV + kvh) * SK + (size_t)s * (SK / NSPLIT) + (size_t)wave * PAGE + r) * DH + 8 * c;
    const u32x4* kp = reinterpret_cast<const u32x4*>(kc + base);
    const u32x4* vp = reinterpret_cast<const u32x4*>(vc + base);
    u32x4 Kn[4], Vn[4];
    const int rot = (s * 2 + kvh * 7 + b * 13);
#pragma unroll
    for (int t = 0; t < 4; ++t) { Kn[t] = __builtin_nontemporal_load(kp + (rot & (PPW - 1)) * (256 * WAVES) + t * 64); Vn[t] = __builtin_nontemporal_load(vp + (rot & (PPW - 1)) * (256 * WAVES) + t * 64); }
    u32x4 q0, q1;
    {
      const float* qp = reinterpret_cast<const float*>(ws + WS_Q) + b * QN + kvh * 2 * DH + 8 * c;
      f32x4 s0 = {0.f, 0.f, 0.f, 0.f}, s1 = s0, s2 = s0, s3 = s0;
#pragma unroll
      for (int k = 0; k < KS; ++k) {
        const float* p = qp + (size_t)k * NB * QN;
        s0 += *reinterpret_cast<const f32x4*>(p); s1 += *reinterpret_cast<const f32x4*>(p + 4);
        s2 += *reinterpret_cast<const f32x4*>(p + DH); s3 += *reinterpret_cast<const f32x4*>(p + DH + 4);
      }
      q0 = u32x4{f2bf(s0[0]) | ((unsigned)f2bf(s0[1]) << 16), f2bf(s0[2]) | ((unsigned)f2bf(s0[3]) << 16), f2bf(s1[0]) | ((unsigned)f2bf(s1[1]) << 16), f2bf(s1[2]) | ((unsigned)f2bf(s1[3]) << 16)};
      q1 = u32x4{f2bf(s2[0]) | ((unsigned)f2bf(s2[1]) << 16), f2bf(s2[2]) | ((unsigned)f2bf(s2[3]) << 16), f2bf(s3[0]) | ((unsigned)f2bf(s3[1]) << 16), f2bf(s3[2]) | ((unsigned)f2bf(s3[3]) << 16)};
    }
    float m0 = -INFINITY, m1 = -INFINITY, l0 = 0.f, l1 = 0.f, acc0[8], acc1[8];
#pragma unroll
    for (int k = 0; k < 8; ++k) { acc0[k] = 0.f; acc1[k] = 0.f; }
    for (int i = 0; i < PPW; ++i) {
      u32x4 Kc[4], Vc[4];
#pragma unroll
      for (int t = 0; t < 4; ++t) { Kc[t] = Kn[t]; Vc[t] = Vn[t]; }
      if (i + 1 < PPW) {
#pragma unroll
        for (int t = 0; t < 4; ++t) { Kn[t] = __builtin_nontemporal_load(kp + ((i + 1 + rot) & (PPW - 1)) * (256 * WAVES) + t * 64); Vn[t] = __builtin_nontemporal_load(vp + ((i + 1 + rot) & (PPW - 1)) * (256 * WAVES) + t * 64); }
      }
      float x0[4], x1[4];
#pragma unroll
      for (int t = 0; t < 4; ++t) {
        float d0 = 0.f, d1 = 0.f;
#pragma unroll
        for (int k = 0; k < 4; ++k) { d0 = dot2(Kc[t][k], q0[k], d0); d1 = dot2(Kc[t][k], q1[k], d1); }
        x0[t] = row16_allsum(d0) * C2; x1[t] = row16_allsum(d1) * C2;
      }
      const float mn0 = fmaxf(fmaxf(fmaxf(x0[0], x0[1]), fmaxf(x0[2], x0[3])), m0);
      const float mn1 = fmaxf(fmaxf(fmaxf(x1[0], x1[1]), fmaxf(x1[2], x1[3])), m1);
      const float al0 = __builtin_amdgcn_exp2f(m0 - mn0), al1 = __builtin_amdgcn_exp2f(m1 - mn1);
      m0 = mn0; m1 = mn1;
      float p0[4], p1[4];
#pragma unroll
      for (int t = 0; t < 4; ++t) { p0[t] = __builtin_amdgcn_exp2f(x0[t] - mn0); p1[t] = __builtin_amdgcn_exp2f(x1[t] - mn1); }
      l0 = fmaf(l0, al0, (p0[0] + p0[1]) + (p0[2] + p0[3]));
      l1 = fmaf(l1, al1, (p1[0] + p1[1]) + (p1[2] + p1[3]));
#pragma unroll
      for (int k = 0; k < 8; ++k) { acc0[k] *= al0; acc1[k] *= al1; }
#pragma unroll
      for (int t = 0; t < 4; ++t) {
#pragma unroll
        for (int k = 0; k < 4; ++k) {
          const float lo = bf_lo(Vc[t][k]), hi = bf_hi(Vc[t][k]);
          acc0[2 * k] = fmaf(p0[t], lo, acc0[2 * k]); acc0[2 * k + 1] = fmaf(p0[t], hi, acc0[2 * k + 1]);
          acc1[2 * k] = fmaf(p1[t], lo, acc1[2 * k]); acc1[2 * k + 1] = fmaf(p1[t], hi, acc1[2 * k + 1]);
        }
      }
    }
    {
      float* rec0 = &mrg[wave * 4 + r][0][0];
      float* rec1 = &mrg[wave * 4 + r][1][0];
      *reinterpret_cast<f32x4*>(rec0 + 4 + 8 * c) = f32x4{acc0[0], acc0[1], acc0[2], acc0[3]};
      *reinterpret_cast<f32x4*>(rec0 + 8 + 8 * c) = f32x4{acc0[4], acc0[5], acc0[6], acc0[7]};
      *reinterpret_cast<f32x4*>(rec1 + 4 + 8 * c) = f32x4{acc1[0], acc1[1], acc1[2], acc1[3]};
      *reinterpret_cast<f32x4*>(rec1 + 8 + 8 * c) = f32x4{acc1[4], acc1[5], acc1[6], acc1[7]};
      if (c == 0) { rec0[0] = m0; rec0[1] = l0; rec1[0] = m1; rec1[1] = l1; }
    }
    __syncthreads();
    if (tid < 256) {
      const int g = tid >> 7, d = tid & 127;
      float M = -INFINITY;
#pragma unroll
      for (int w = 0; w < 4 * WAVES; ++w) M = fmaxf(M, mrg[w][g][0]);
      float L = 0.f, o = 0.f;
#pragma unroll
      for (int w = 0; w < 4 * WAVES; ++w) { const float wt = __builtin_amdgcn_exp2f(mrg[w][g][0] - M); L = fmaf(wt, mrg[w][g][1], L); o = fmaf(wt, mrg[w][g][4 + d], o); }
      const size_t slot = ((size_t)(b * HQ + kvh * 2 + g)) * NSPLIT + s;
      st_dev(reinterpret_cast<float*>(ws + WS_PO) + slot * DH + d, o);
      if (d == 0) { float* pml = reinterpret_cast<float*>(ws + WS_PML) + slot * 2; st_dev(pml, M); st_dev(pml + 1, L); }
    }
    asm volatile("s_waitcnt vmcnt(0)" ::: "memory");
    __syncthreads();
    if (tid == 0) __hip_atomic_fetch_add(cnts + (3 + kvh) * 16, 1u, __ATOMIC_RELAXED, __HIP_MEMORY_SCOPE_AGENT);
    return;
  }
  float (*vin)[128] = reinterpret_cast<float (*)[128]>(smem);
  float (*red)[NB][192] = reinterpret_cast<float (*)[NB][192]>(smem + 256);
  __shared__ float gsh[NB];
  const int fid = blockIdx.x - N_ATT, kb = fid / 3, cgp = fid % 3;
  const int cl = tid % 24, rl = tid / 24;
  u32x4 wv[16];
  if (tid < 192) {
    const unsigned short* w = (kb < 32 ? wo + (size_t)kb * 128 * DM : wd + (size_t)(kb - 32) * 128 * DM) + cgp * 192 + cl * 8;
#pragma unroll
    for (int i = 0; i < 16; ++i) wv[i] = *reinterpret_cast<const u32x4*>(w + (size_t)(rl + 8 * i) * DM);
  }
  {
    const int wave = tid >> 6, lane = tid & 63;
    if (wave >= 2 && wave < 4) {
      float sum = 0.f;
#pragma unroll
      for (int k = 0; k < DM / 64; ++k) sum += rbf(bf2f(x[(wave - 2) * DM + k * 64 + lane]) * bf2f(gw[k * 64 + lane]));
      for (int o = 32; o >= 1; o >>= 1) sum += __shfl_xor(sum, o);
      if (lane == 0) gsh[wave - 2] = rbf(1.f / (1.f + __expf(-rbf(sum))));
    }
  }
  if (kb < 32) {
    if (tid == 0) {
      const unsigned* cnt = cnts + (3 + (kb >> 1)) * 16;
      for (int it = 0; it < (1 << 22); ++it) {
        if (__hip_atomic_load(cnt, __ATOMIC_RELAXED, __HIP_MEMORY_SCOPE_AGENT) >= (unsigned)(NB * NSPLIT)) break;
        __builtin_amdgcn_s_sleep(8);
      }
      __builtin_amdgcn_fence(__ATOMIC_ACQUIRE, "agent");
    }
    __syncthreads();
  }
  if (tid < 256) {
    const int e = tid, b = e >> 7, d = e & 127;
    float v;
    if (kb < 32) {
      const size_t row = (size_t)(b * HQ + kb) * NSPLIT;
      const float* pml = reinterpret_cast<const float*>(ws + WS_PML) + row * 2;
      const float* po = reinterpret_cast<const float*>(ws + WS_PO) + row * DH + d;
      float M = -INFINITY;
#pragma unroll
      for (int s = 0; s < NSPLIT; ++s) M = fmaxf(M, pml[2 * s]);
      float L = 0.f, o = 0.f;
#pragma unroll
      for (int s = 0; s < NSPLIT; ++s) { const float wt = __builtin_amdgcn_exp2f(pml[2 * s] - M); L = fmaf(wt, pml[2 * s + 1], L); o = fmaf(wt, po[(size_t)s * DH], o); }
      v = rbf(o / L);
    } else {
      const int f = (kb - 32) * 128 + d;
      float gs = 0.f, us = 0.f;
#pragma unroll
      for (int k = 0; k < KS; ++k) { gs += reinterpret_cast<const float*>(ws + WS_G)[(size_t)(k * NB + b) * DF + f]; us += reinterpret_cast<const float*>(ws + WS_U)[(size_t)(k * NB + b) * DF + f]; }
      const float g = rbf(gs), u = rbf(us);
      const float t = tanhf(0.7978845608028654f * (g + 0.044715f * g * g * g));
      const float ge = rbf(0.5f * g * (1.f + t));
      v = rbf(ge * u);
    }
    vin[b][d] = v;
  }
  __syncthreads();
  if (tid < 192) {
    float a0[8], a1[8];
#pragma unroll
    for (int j = 0; j < 8; ++j) { a0[j] = 0.f; a1[j] = 0.f; }
#pragma unroll
    for (int i = 0; i < 16; ++i) {
      const float x0 = vin[0][rl + 8 * i], x1 = vin[1][rl + 8 * i];
#pragma unroll
      for (int j = 0; j < 4; ++j) {
        const float lo = bf_lo(wv[i][j]), hi = bf_hi(wv[i][j]);
        a0[2 * j] = fmaf(x0, lo, a0[2 * j]); a0[2 * j + 1] = fmaf(x0, hi, a0[2 * j + 1]);
        a1[2 * j] = fmaf(x1, lo, a1[2 * j]); a1[2 * j + 1] = fmaf(x1, hi, a1[2 * j + 1]);
      }
    }
#pragma unroll
    for (int j = 0; j < 8; ++j) { red[rl][0][cl * 8 + j] = a0[j]; red[rl][1][cl * 8 + j] = a1[j]; }
  }
  __syncthreads();
  float* fp = reinterpret_cast<float*>(ws + WS_FP);
  if (tid < 192) {
    float s0 = 0.f, s1 = 0.f;
#pragma unroll
    for (int k = 0; k < 8; ++k) { s0 += red[k][0][tid]; s1 += red[k][1][tid]; }
    st_dev(fp + (size_t)(kb * NB + 0) * DM + cgp * 192 + tid, s0);
    st_dev(fp + (size_t)(kb * NB + 1) * DM + cgp * 192 + tid, s1);
  }
  asm volatile("s_waitcnt vmcnt(0)" ::: "memory");
  __syncthreads();
  if (tid == 0) {
    const unsigned old = __hip_atomic_fetch_add(cnts + cgp * 16, 1u, __ATOMIC_RELAXED, __HIP_MEMORY_SCOPE_AGENT);
    const unsigned last = (old == 67u) ? 1u : 0u;
    if (last) __builtin_amdgcn_fence(__ATOMIC_ACQUIRE, "agent");
    flag = last;
  }
  __syncthreads();
  if (flag == 0u) return;
  for (int e = tid; e < NB * 192; e += 512) {
    const int b = e / 192, n = cgp * 192 + e % 192;
    float v[68];
#pragma unroll
    for (int k = 0; k < 68; ++k) v[k] = fp[(size_t)(k * NB + b) * DM + n];
    float a = 0.f, bb = 0.f;
#pragma unroll
    for (int k = 0; k < 32; ++k) a += v[k];
#pragma unroll
    for (int k = 32; k < 68; ++k) bb += v[k];
    const float g = gsh[b];
    const float r1 = rbf(g * rbf(a)), r2 = rbf(rbf(1.f - g) * rbf(bb));
    out[b * DM + n] = f2bf(r1 + r2);
  }
}

extern "C" void kernel_launch(void* const* d_in, const int* in_sizes, int n_in, void* d_out, int out_size, void* d_ws, size_t ws_size,
                              hipStream_t stream) {
  const unsigned short* x = (const unsigned short*)d_in[0];
  const unsigned short* kc = (const unsigned short*)d_in[1];
  const unsigned short* vc = (const unsigned short*)d_in[2];
  const unsigned short* wq = (const unsigned short*)d_in[3];
  const unsigned short* wo = (const unsigned short*)d_in[4];
  const unsigned short* wg = (const unsigned short*)d_in[6];
  const unsigned short* wu = (const unsigned short*)d_in[7];
  const unsigned short* wd = (const unsigned short*)d_in[8];
  const unsigned short* gw = (const unsigned short*)d_in[9];
  unsigned char* ws = (unsigned char*)d_ws;
  proj_kernel<<<dim3(QN / 512 + 2 * (DF / 512), KS), dim3(256), 0, stream>>>(x, wq, wg, wu, ws);
  main_kernel<<<dim3(N_ATT + N_FIN), dim3(512), 0, stream>>>(kc, vc, x, wo, wd, gw, ws, (unsigned short*)d_out);
}
```
